```python
import torch

def _softmax(x, axis=-1):
    return torch.softmax(x, dim=axis)
def _gelu(x):
    return torch.nn.functional.gelu(x, approximate="tanh")
def _silu(x):
    return torch.nn.functional.silu(x)
def _sigmoid(x):
    return torch.sigmoid(x)
def _layernorm(x, axis=-1, eps=1e-5):
    m = x.mean(dim=axis, keepdim=True)
    v = x.var(dim=axis, keepdim=True, unbiased=False)
    return (x - m) / torch.sqrt(v + eps)
def _scatter_add(tgt, idx, src, axis=0):
    return tgt.clone().index_add_(axis, idx, src)
def _segment_sum(src, seg, n):
    out = torch.zeros((n,) + src.shape[1:], dtype=src.dtype)
    return out.index_add_(0, seg, src)
def _pad(x, pw):
    flat = tuple(v for pair in reversed(pw) for v in pair)
    return torch.nn.functional.pad(x, flat)

kernel_name = 'parallel_sum__spectral_fnet__geglu_ffn__prompt'

def setup_inputs():

    _w = (lambda a: (lambda t: t.to(torch.bfloat16) if t.is_floating_point() else t)(torch.as_tensor(a)))
    def _A_setup():
        B, S, D = 1, 2048, 512
        ns = torch.arange(S)[:, None] * torch.arange(S)[None, :]
        nd = torch.arange(D)[:, None] * torch.arange(D)[None, :]
        Ws_re = torch.cos(-2.0 * torch.pi * ns / S).to(float)
        Ws_im = torch.sin(-2.0 * torch.pi * ns / S).to(float)
        Wd_re = torch.cos(-2.0 * torch.pi * nd / D).to(float)
        Wd_im = torch.sin(-2.0 * torch.pi * nd / D).to(float)
        return {
            "x": _w((lambda *s: torch.randn(*s, dtype=torch.bfloat16) * 0.05)(B, S, D)),
            "dft_seq_re": _w(Ws_re),
            "dft_seq_im": _w(Ws_im),
            "dft_hid_re": _w(Wd_re),
            "dft_hid_im": _w(Wd_im),
        }
    _A_in = _A_setup()

    def _B_setup():
        B, S, D, F = 1, 2048, 512, 1376
        return {
            "x": _w((lambda *s: torch.randn(*s, dtype=torch.bfloat16) * 0.05)(B, S, D)),
            "w_gate": _w((lambda *s: torch.randn(*s, dtype=torch.bfloat16) * 0.2)(D, F)),
            "w_up": _w((lambda *s: torch.randn(*s, dtype=torch.bfloat16) * 0.2)(D, F)),
            "w_down": _w((lambda *s: torch.randn(*s, dtype=torch.bfloat16) * 0.2)(F, D)),
        }
    _B_in = _B_setup()

    return {
        "A_x": _A_in["x"],
        "A_dft_seq_re": _A_in["dft_seq_re"],
        "A_dft_seq_im": _A_in["dft_seq_im"],
        "A_dft_hid_re": _A_in["dft_hid_re"],
        "A_dft_hid_im": _A_in["dft_hid_im"],
        "B_x": _B_in["x"],
        "B_w_gate": _B_in["w_gate"],
        "B_w_up": _B_in["w_up"],
        "B_w_down": _B_in["w_down"],
    }

def reference(A_x, A_dft_seq_re, A_dft_seq_im, A_dft_hid_re, A_dft_hid_im, B_x, B_w_gate, B_w_up, B_w_down):
    def _A_ref(x, dft_seq_re, dft_seq_im, dft_hid_re, dft_hid_im):
        a = torch.einsum("bsd,ed->bse", x, dft_hid_re)
        b = torch.einsum("bsd,ed->bse", x, dft_hid_im)
        y = torch.einsum("ts,bse->bte", dft_seq_re, a) - torch.einsum("ts,bse->bte", dft_seq_im, b)
        return y

    def _B_ref(x, w_gate, w_up, w_down):
        return (_gelu(x @ w_gate) * (x @ w_up)) @ w_down

    x = A_x
    xn = _layernorm(x, axis=-1)
    return x + _A_ref(xn, A_dft_seq_re, A_dft_seq_im, A_dft_hid_re, A_dft_hid_im) + _B_ref(xn, B_w_gate, B_w_up, B_w_down)
```

```cpp
#include <hip/hip_runtime.h>
#include <hip/hip_bf16.h>
#include <stdint.h>

typedef __attribute__((ext_vector_type(8))) short bf16x8;
typedef __attribute__((ext_vector_type(4))) short s16x4;
typedef __attribute__((ext_vector_type(4))) float f32x4;
typedef __attribute__((ext_vector_type(2))) float f32x2;
typedef __attribute__((ext_vector_type(4))) unsigned u32x4;
typedef __attribute__((ext_vector_type(2))) unsigned u32x2;
typedef __attribute__((ext_vector_type(2))) __bf16 bf2_t;
typedef unsigned short u16;
typedef __amdgpu_buffer_rsrc_t rsrc_t;

#define LAS __attribute__((address_space(3)))
#define GAS __attribute__((address_space(1)))

constexpr int S_ = 2048, D_ = 512, F_ = 1376;
constexpr int HLD = 2048;

constexpr size_t WS_AB = 0;
constexpr size_t WS_H = 4u << 20;
constexpr size_t WS_SLAB = 16u << 20;
constexpr size_t WS_CNT = 48u << 20;

#define WAIT_V(n) asm volatile("s_waitcnt vmcnt(" #n ")" ::: "memory")
#define WAIT_L0() asm volatile("s_waitcnt lgkmcnt(0)" ::: "memory")
#define BAR() __builtin_amdgcn_s_barrier()

__device__ __forceinline__ unsigned pack_bf16(float a, float b) {
  f32x2 v = {a, b};
  bf2_t r = __builtin_convertvector(v, bf2_t);
  return __builtin_bit_cast(unsigned, r);
}
__device__ __forceinline__ float bf_lo(unsigned u) { return __builtin_bit_cast(float, u << 16); }
__device__ __forceinline__ float bf_hi(unsigned u) { return __builtin_bit_cast(float, u & 0xffff0000u); }

__device__ __forceinline__ rsrc_t mk_rsrc(const void* p, unsigned bytes) {
  return __builtin_amdgcn_make_buffer_rsrc((void*)p, (short)0, (int)bytes, 0x00020000);
}
__device__ __forceinline__ void dma16(unsigned m0v, unsigned voff, rsrc_t rs, unsigned soff) {
  asm volatile("s_mov_b32 m0, %0\n\ts_nop 0\n\tbuffer_load_dwordx4 %1, %2, %3 offen lds"
               :: "s"(m0v), "v"(voff), "s"(rs), "s"(soff) : "memory");
}
__device__ __forceinline__ s16x4 tr_read(const char* p) {
  typedef __attribute__((ext_vector_type(4))) short v4i16_t;
  return __builtin_amdgcn_ds_read_tr16_b64_v4i16((LAS v4i16_t*)p);
}
__device__ __forceinline__ bf16x8 cat8(s16x4 a, s16x4 b) {
  return __builtin_shufflevector(a, b, 0, 1, 2, 3, 4, 5, 6, 7);
}
__device__ __forceinline__ float gelu_tanh(float x) {
  float u = x * (0.7978845608028654f + 0.035677408136300125f * x * x);
  float e = __builtin_amdgcn_exp2f(-2.885390081777927f * u);
  return x * __builtin_amdgcn_rcpf(1.0f + e);
}

constexpr int K1_A_SLOT = 16384, K1_B_SLOT = 32768, K1_B_OFF = 2 * K1_A_SLOT;
constexpr int K1_LDS = K1_B_OFF + 3 * K1_B_SLOT;

template <bool TR>
__device__ __forceinline__ void k1_body(char* smem, const u16* __restrict__ x, const u16* __restrict__ w0,
                                        const u16* __restrict__ w1, u16* __restrict__ outp, int rb, int ncol0,
                                        float sgn) {
  const int tid = threadIdx.x;
  const int lane = tid & 63, wid = __builtin_amdgcn_readfirstlane(tid >> 6), wm = wid >> 2, wn = wid & 3;
  const int fr = lane & 15, fq = lane >> 4;
  const unsigned ldsb = (unsigned)(size_t)(LAS char*)smem;

  const int j8 = tid & 7, r0 = tid >> 3;
  u32x4 xv[2][8];
  {
    const u16* xp = x + (size_t)(rb * 128 + r0) * D_ + j8 * 8;
#pragma unroll
    for (int rr = 0; rr < 2; ++rr)
#pragma unroll
      for (int c = 0; c < 8; ++c) xv[rr][c] = *(const u32x4*)(xp + rr * 64 * D_ + c * 64);
  }
  unsigned bvoff;
  rsrc_t rs0, rs1;
  if constexpr (!TR) {
    const int row = tid >> 3, pos = tid & 7, sc = pos ^ ((row >> 1) & 7);
    bvoff = row * (D_ * 2) + sc * 16;
    rs0 = mk_rsrc(w0, D_ * D_ * 2);
    rs1 = rs0;
  } else {
    const int k = tid >> 4, p16 = tid & 15;
    const int f = (k & 3) | (((k >> 3) & 1) << 2);
    bvoff = k * (F_ * 2) + ((p16 ^ (f << 1)) * 16);
    rs0 = mk_rsrc(w0, D_ * F_ * 2);
    rs1 = mk_rsrc(w1, D_ * F_ * 2);
  }
  auto stageB = [&](int c, int slot) {
    const unsigned m0b = ldsb + K1_B_OFF + slot * K1_B_SLOT + wid * 1024;
#pragma unroll
    for (int i = 0; i < 4; ++i) {
      if constexpr (!TR) dma16(m0b + i * 8192, bvoff, rs0, (ncol0 + 64 * i) * (D_ * 2) + c * 128);
      else dma16(m0b + i * 8192, bvoff, i < 2 ? rs0 : rs1, (c * 64 + 32 * (i & 1)) * (F_ * 2) + ncol0 * 2);
    }
  };
  stageB(0, 0);
  stageB(1, 1);

  float mean[2], rstd[2];
#pragma unroll
  for (int rr = 0; rr < 2; ++rr) {
    float s = 0.f;
#pragma unroll
    for (int c = 0; c < 8; ++c)
#pragma unroll
      for (int e = 0; e < 4; ++e) s += bf_lo(xv[rr][c][e]) + bf_hi(xv[rr][c][e]);
    s += __shfl_xor(s, 1);
    s += __shfl_xor(s, 2);
    s += __shfl_xor(s, 4);
    const float m = s * (1.0f / D_);
    float q = 0.f;
#pragma unroll
    for (int c = 0; c < 8; ++c)
#pragma unroll
      for (int e = 0; e < 4; ++e) {
        float a = bf_lo(xv[rr][c][e]) - m, b = bf_hi(xv[rr][c][e]) - m;
        q += a * a;
        q += b * b;
      }
    q += __shfl_xor(q, 1);
    q += __shfl_xor(q, 2);
    q += __shfl_xor(q, 4);
    mean[rr] = m;
    rstd[rr] = __builtin_amdgcn_rsqf(q * (1.0f / D_) + 1e-5f);
  }
  const int awoff = r0 * 128 + ((j8 ^ ((r0 >> 1) & 7)) << 4);
  auto writeA = [&](int c, int slot) {
#pragma unroll
    for (int rr = 0; rr < 2; ++rr) {
      u32x4 o;
#pragma unroll
      for (int e = 0; e < 4; ++e)
        o[e] = pack_bf16((bf_lo(xv[rr][c][e]) - mean[rr]) * rstd[rr], (bf_hi(xv[rr][c][e]) - mean[rr]) * rstd[rr]);
      *(u32x4*)(smem + slot * K1_A_SLOT + awoff + rr * 64 * 128) = o;
    }
  };
  writeA(0, 0);

  const int asw = (fr >> 1) & 7;
  const int a_rd0 = (wm * 64 + fr) * 128 + ((fq ^ asw) << 4);
  int b_rd[2];
  if constexpr (!TR) {
    b_rd[0] = K1_B_OFF + (wn * 64 + fr) * 128 + ((fq ^ asw) << 4);
    b_rd[1] = b_rd[0] ^ 64;
  } else {
    const int q = fr >> 2, pp = fr & 3, f = q | ((fq & 1) << 2);
#pragma unroll
    for (int jj = 0; jj < 2; ++jj) {
      const int ch = 4 * wn + 2 * jj + (pp >> 1);
      b_rd[jj] = K1_B_OFF + (8 * fq + q) * 256 + ((ch ^ (f << 1)) << 4) + (pp & 1) * 8;
    }
  }

  f32x4 acc[4][4];
#pragma unroll
  for (int j = 0; j < 4; ++j)
#pragma unroll
    for (int i = 0; i < 4; ++i) acc[j][i] = (f32x4){0.f, 0.f, 0.f, 0.f};

#pragma unroll
  for (int c = 0; c < 8; ++c) {
    if (c < 7) WAIT_V(4); else WAIT_V(0);
    WAIT_L0();
    BAR();
    if (c + 2 < 8) stageB(c + 2, (c + 2) % 3);
    if (c + 1 < 8) writeA(c + 1, (c + 1) & 1);
    const char* As = smem + (c & 1) * K1_A_SLOT;
    const char* Bs = smem + (c % 3) * K1_B_SLOT;
#pragma unroll
    for (int ks = 0; ks < 2; ++ks) {
      bf16x8 af[4], bfr[4];
#pragma unroll
      for (int i = 0; i < 4; ++i) af[i] = *(const bf16x8*)(As + (a_rd0 ^ (ks * 64)) + i * 2048);
#pragma unroll
      for (int j = 0; j < 4; ++j) {
        if constexpr (!TR) {
          bfr[j] = *(const bf16x8*)(Bs + b_rd[ks] + j * 2048);
        } else {
          const char* p = Bs + b_rd[j & 1] + (j >> 1) * 16384 + ks * 32 * 256;
          bfr[j] = cat8(tr_read(p), tr_read(p + 4 * 256));
        }
      }
#pragma unroll
      for (int j = 0; j < 4; ++j)
#pragma unroll
        for (int i = 0; i < 4; ++i)
          acc[j][i] = __builtin_amdgcn_mfma_f32_16x16x32_bf16(bfr[j], af[i], acc[j][i], 0, 0, 0);
    }
  }

  if constexpr (!TR) {
#pragma unroll
    for (int i = 0; i < 4; ++i) {
      u16* op = outp + (size_t)(rb * 128 + wm * 64 + i * 16 + fr) * D_ + ncol0 + wn * 64 + fq * 4;
#pragma unroll
      for (int j = 0; j < 4; ++j) {
        u32x2 o;
        o[0] = pack_bf16(acc[j][i][0] * sgn, acc[j][i][1] * sgn);
        o[1] = pack_bf16(acc[j][i][2] * sgn, acc[j][i][3] * sgn);
        *(u32x2*)(op + j * 16) = o;
      }
    }
  } else {
    const int fcol = ncol0 + wn * 32;
    const bool valid = fcol < F_;
#pragma unroll
    for (int i = 0; i < 4; ++i) {
      u16* op = outp + (size_t)(rb * 128 + wm * 64 + i * 16 + fr) * HLD + fcol + fq * 4;
#pragma unroll
      for (int j = 0; j < 2; ++j) {
        u32x2 o = {0u, 0u};
        if (valid) {
          float v[4];
#pragma unroll
          for (int r = 0; r < 4; ++r) v[r] = gelu_tanh(acc[j][i][r]) * acc[j + 2][i][r];
          o[0] = pack_bf16(v[0], v[1]);
          o[1] = pack_bf16(v[2], v[3]);
        }
        *(u32x2*)(op + j * 16) = o;
      }
    }
  }
}

__global__ __launch_bounds__(512, 2) void k1_ln_gemm(const u16* __restrict__ x, const u16* __restrict__ wr,
                                                     const u16* __restrict__ wi, const u16* __restrict__ wg,
                                                     const u16* __restrict__ wu, u16* __restrict__ ab,
                                                     u16* __restrict__ h, unsigned* __restrict__ cnt) {
  extern __shared__ __attribute__((aligned(16))) char smem[];
  const int bid = blockIdx.x;
  if (bid == 0 && threadIdx.x < 64) cnt[threadIdx.x * 32] = 0u;
  const int id = (bid & 7) * 30 + (bid >> 3);
  const int ct = id >> 4, rb = id & 15;
  if (ct < 4) {
    k1_body<false>(smem, x, ct < 2 ? wr : wi, nullptr, ab + (ct < 2 ? 0 : (size_t)S_ * D_), rb, (ct & 1) * 256,
                   ct < 2 ? 1.0f : -1.0f);
  } else {
    k1_body<true>(smem, x, wg, wu, h, rb, (ct - 4) * 128, 1.0f);
  }
}

constexpr int K2_SLOT = 32768, K2_LDS = 4 * K2_SLOT;

__device__ __forceinline__ void k2_stage(int c, unsigned m0b, const u16* cr, const u16* ci, const u16* h,
                                         const u16* ab, const u16* wd, unsigned avoff, unsigned bvoff,
                                         unsigned asoff0, unsigned bsoff0) {
  const u16* ap = c < 32 ? cr : (c < 64 ? ci : h);
  const rsrc_t ra = mk_rsrc(ap, S_ * S_ * 2);
  const unsigned asoff = asoff0 + (unsigned)(c & 31) * 128u;
  const rsrc_t rb = mk_rsrc(c < 64 ? ab : wd, c < 64 ? 2 * S_ * D_ * 2 : F_ * D_ * 2);
  const unsigned bsoff = bsoff0 + (unsigned)(c & 63) * (64u * 1024u);
#pragma unroll
  for (int i = 0; i < 4; ++i) dma16(m0b + i * 4096, avoff, ra, asoff + i * (32u * 4096u));
#pragma unroll
  for (int i = 0; i < 4; ++i) dma16(m0b + 16384 + i * 4096, bvoff, rb, bsoff + i * (16u * 1024u));
}

__global__ __launch_bounds__(256, 1) void k2_gemm(const u16* __restrict__ cr, const u16* __restrict__ ci,
                                                  const u16* __restrict__ h, const u16* __restrict__ ab,
                                                  const u16* __restrict__ wd, const u16* __restrict__ x,
                                                  float* __restrict__ slab, unsigned* __restrict__ cnt,
                                                  u16* __restrict__ out) {
  extern __shared__ __attribute__((aligned(16))) char smem[];
  const int tid = threadIdx.x, lane = tid & 63, wid = __builtin_amdgcn_readfirstlane(tid >> 6);
  const int wt = wid >> 1, we = wid & 1;
  const int fr = lane & 15, fq = lane >> 4;
  const unsigned ldsb = (unsigned)(size_t)(LAS char*)smem;
  const int bid = blockIdx.x;
  const int xcd = bid & 7, loc = bid >> 3;
  const int p = xcd >> 1;
  const int trow = (xcd & 1) * 8 + (loc >> 2), tcol = loc & 3;
  const int tile = trow * 4 + tcol;
  const bool full = p < 2;
  const int c0 = p < 2 ? p * 22 : 44 + (p - 2) * 21 - 1;

  const unsigned avoff = (unsigned)(tid >> 3) * 4096u + (((tid & 7) ^ ((tid >> 4) & 7)) * 16);
  const int bk = tid >> 4;
  const int bf_ = (bk & 3) | (((bk >> 3) & 1) << 2);
  const unsigned bvoff = (unsigned)bk * 1024u + (((tid & 15) ^ (bf_ << 1)) * 16);

  const unsigned m0w = ldsb + wid * 1024;
  const unsigned asoff0 = (unsigned)trow * (128u * 4096u), bsoff0 = (unsigned)tcol * 256u;
#define stage(pos, slot) k2_stage(c0 + (pos), m0w + (slot) * K2_SLOT, cr, ci, h, ab, wd, avoff, bvoff, asoff0, bsoff0)

  const int asw = (fr >> 1) & 7;
  int a_rd[4];
#pragma unroll
  for (int i = 0; i < 4; ++i) a_rd[i] = (wt * 64 + ((i + p) & 3) * 16 + fr) * 128 + ((fq ^ asw) << 4);
  int b_rd[4];
  {
    const int q = fr >> 2, pp = fr & 3, f = q | ((fq & 1) << 2);
#pragma unroll
    for (int j = 0; j < 4; ++j) {
      const int ch = 8 * we + 2 * j + (pp >> 1);
      b_rd[j] = 16384 + (8 * fq + q) * 256 + ((ch ^ (f << 1)) << 4) + (pp & 1) * 8;
    }
  }

  f32x4 acc[4][4];
#pragma unroll
  for (int j = 0; j < 4; ++j)
#pragma unroll
    for (int i = 0; i < 4; ++i) acc[j][i] = (f32x4){0.f, 0.f, 0.f, 0.f};

  bf16x8 af0[4], bf0[4], af1[4], bf1[4];
  auto rdfrag = [&](bf16x8* af, bf16x8* bfr, int slot, int ks) {
    const char* base = smem + slot * K2_SLOT;
#pragma unroll
    for (int i = 0; i < 4; ++i) af[i] = *(const bf16x8*)(base + (a_rd[i] ^ (ks * 64)));
#pragma unroll
    for (int j = 0; j < 4; ++j) {
      const char* pb = base + b_rd[j] + ks * 32 * 256;
      bfr[j] = cat8(tr_read(pb), tr_read(pb + 4 * 256));
    }
  };
  auto mma = [&](const bf16x8* af, const bf16x8* bfr) {
#pragma unroll
    for (int j = 0; j < 4; ++j)
#pragma unroll
      for (int i = 0; i < 4; ++i)
        acc[j][i] = __builtin_amdgcn_mfma_f32_16x16x32_bf16(bfr[j], af[i], acc[j][i], 0, 0, 0);
  };
#define K2_POS(j, slot, DO_STAGE)                      \
  do {                                                 \
    if (DO_STAGE) stage((j) + 3, ((slot) + 3) & 3);    \
    rdfrag(af1, bf1, (slot), 1);                       \
    mma(af0, bf0);                                     \
    rdfrag(af0, bf0, ((slot) + 1) & 3, 0);             \
    mma(af1, bf1);                                     \
  } while (0)

  if (full) {
    stage(0, 0);
    stage(1, 1);
    stage(2, 2);
    WAIT_V(8); BAR();
    rdfrag(af0, bf0, 0, 0);
    K2_POS(0, 0, true);
    WAIT_V(8); WAIT_L0(); BAR();
  } else {
    stage(1, 1);
    stage(2, 2);
    stage(3, 3);
    WAIT_V(8); BAR();
    rdfrag(af0, bf0, 1, 0);
  }
  for (int j = 1; j < 17; j += 4) {
    K2_POS(j, 1, true);     WAIT_V(8); WAIT_L0(); BAR();
    K2_POS(j + 1, 2, true); WAIT_V(8); WAIT_L0(); BAR();
    K2_POS(j + 2, 3, true); WAIT_V(8); WAIT_L0(); BAR();
    K2_POS(j + 3, 0, true); WAIT_V(8); WAIT_L0(); BAR();
  }
  K2_POS(17, 1, true);  WAIT_V(8); WAIT_L0(); BAR();
  K2_POS(18, 2, true);  WAIT_V(8); WAIT_L0(); BAR();
  K2_POS(19, 3, false); WAIT_V(0); WAIT_L0(); BAR();
  K2_POS(20, 0, false);
  rdfrag(af1, bf1, 1, 1);
  mma(af0, bf0);
  mma(af1, bf1);

  const rsrc_t rs = mk_rsrc(slab, 64 * 16 * 4096 * 4);
  const unsigned lane_off = (unsigned)(wid * 4 * 64 + lane) * 16;
#pragma unroll
  for (int i = 1; i < 4; ++i) {
    const unsigned dstq = (i + p) & 3;
    const unsigned off = ((tile * 4 + dstq) * 4 + p) * 16384u + lane_off;
#pragma unroll
    for (int j = 0; j < 4; ++j)
      __builtin_amdgcn_raw_buffer_store_b128(__builtin_bit_cast(u32x4, acc[j][i]), rs, off + j * 1024, 0, 16);
  }
  asm volatile("s_waitcnt vmcnt(0)" ::: "memory");
  __syncthreads();
  GAS unsigned* cw = (GAS unsigned*)(cnt + tile * 32);
  if (wid == 0) {
    if (lane == 0) __hip_atomic_fetch_add(cw, 1u, __ATOMIC_RELAXED, __HIP_MEMORY_SCOPE_AGENT);
    for (unsigned spins = 0; spins < (1u << 20); ++spins) {
      unsigned v = __hip_atomic_load(cw, __ATOMIC_RELAXED, __HIP_MEMORY_SCOPE_AGENT);
      if ((unsigned)__builtin_amdgcn_readfirstlane(v) >= 4u) break;
      __builtin_amdgcn_s_sleep(1);
    }
  }
  __syncthreads();
  __builtin_amdgcn_fence(__ATOMIC_ACQUIRE, "wavefront");

  const int trw = trow * 128 + wt * 64 + p * 16 + fr;
  const int ecol = tcol * 128 + we * 64 + fq * 4;
  u32x2 xr[4];
#pragma unroll
  for (int j = 0; j < 4; ++j) xr[j] = *(const u32x2*)(x + (size_t)trw * D_ + ecol + j * 16);
  u32x4 pv[3][4];
#pragma unroll
  for (int s = 1; s < 4; ++s) {
    const unsigned srcq = (s + p) & 3;
    const unsigned off = ((tile * 4 + p) * 4 + srcq) * 16384u + lane_off;
#pragma unroll
    for (int j = 0; j < 4; ++j) pv[s - 1][j] = __builtin_amdgcn_raw_buffer_load_b128(rs, off + j * 1024, 0, 16);
  }
#pragma unroll
  for (int j = 0; j < 4; ++j) {
    f32x4 v = acc[j][0];
#pragma unroll
    for (int s = 0; s < 3; ++s) v += __builtin_bit_cast(f32x4, pv[s][j]);
    u32x2 o;
    o[0] = pack_bf16(v[0] + bf_lo(xr[j][0]), v[1] + bf_hi(xr[j][0]));
    o[1] = pack_bf16(v[2] + bf_lo(xr[j][1]), v[3] + bf_hi(xr[j][1]));
    *(u32x2*)(out + (size_t)trw * D_ + ecol + j * 16) = o;
  }
}

extern "C" void kernel_launch(void* const* d_in, const int* in_sizes, int n_in, void* d_out, int out_size,
                              void* d_ws, size_t ws_size, hipStream_t stream) {
  const u16* x = (const u16*)d_in[0];
  const u16* cr = (const u16*)d_in[1];
  const u16* ci = (const u16*)d_in[2];
  const u16* wr = (const u16*)d_in[3];
  const u16* wi = (const u16*)d_in[4];
  const u16* wg = (const u16*)d_in[6];
  const u16* wu = (const u16*)d_in[7];
  const u16* wd = (const u16*)d_in[8];
  char* ws = (char*)d_ws;
  u16* ab = (u16*)(ws + WS_AB);
  u16* h = (u16*)(ws + WS_H);
  float* slab = (float*)(ws + WS_SLAB);
  unsigned* cnt = (unsigned*)(ws + WS_CNT);
  (void)hipFuncSetAttribute((const void*)k1_ln_gemm, hipFuncAttributeMaxDynamicSharedMemorySize, K1_LDS);
  (void)hipFuncSetAttribute((const void*)k2_gemm, hipFuncAttributeMaxDynamicSharedMemorySize, K2_LDS);
  k1_ln_gemm<<<240, 512, K1_LDS, stream>>>(x, wr, wi, wg, wu, ab, h, cnt);
  k2_gemm<<<256, 256, K2_LDS, stream>>>(cr, ci, h, ab, wd, x, slab, cnt, (u16*)d_out);
}
```

```cpp
#include <hip/hip_runtime.h>
#include <hip/hip_bf16.h>
#include <stdint.h>

typedef __attribute__((ext_vector_type(8))) short bf16x8;
typedef __attribute__((ext_vector_type(4))) short s16x4;
typedef __attribute__((ext_vector_type(4))) float f32x4;
typedef __attribute__((ext_vector_type(2))) float f32x2;
typedef __attribute__((ext_vector_type(4))) unsigned u32x4;
typedef __attribute__((ext_vector_type(2))) unsigned u32x2;
typedef __attribute__((ext_vector_type(2))) __bf16 bf2_t;
typedef unsigned short u16;
typedef __amdgpu_buffer_rsrc_t rsrc_t;

#define LAS __attribute__((address_space(3)))
#define GAS __attribute__((address_space(1)))

constexpr int S_ = 2048, D_ = 512, F_ = 1376;
constexpr int HLD = 2048;

constexpr size_t WS_AB = 0;
constexpr size_t WS_H = 4u << 20;
constexpr size_t WS_SLAB = 16u << 20;
constexpr size_t WS_CNT = 48u << 20;

#define WAIT_V(n) asm volatile("s_waitcnt vmcnt(" #n ")" ::: "memory")
#define WAIT_L0() asm volatile("s_waitcnt lgkmcnt(0)" ::: "memory")
#define BAR() __builtin_amdgcn_s_barrier()
#define STAMP(k, i) do {} while (0)
#define CSTAMP(k, i) do {} while (0)

__device__ __forceinline__ unsigned pack_bf16(float a, float b) {
  f32x2 v = {a, b};
  bf2_t r = __builtin_convertvector(v, bf2_t);
  return __builtin_bit_cast(unsigned, r);
}
__device__ __forceinline__ float bf_lo(unsigned u) { return __builtin_bit_cast(float, u << 16); }
__device__ __forceinline__ float bf_hi(unsigned u) { return __builtin_bit_cast(float, u & 0xffff0000u); }

__device__ __forceinline__ rsrc_t mk_rsrc(const void* p, unsigned bytes) {
  return __builtin_amdgcn_make_buffer_rsrc((void*)p, (short)0, (int)bytes, 0x00020000);
}
__device__ __forceinline__ void dma16(unsigned m0v, unsigned voff, rsrc_t rs, unsigned soff) {
  asm volatile("s_mov_b32 m0, %0\n\ts_nop 0\n\tbuffer_load_dwordx4 %1, %2, %3 offen lds"
               :: "s"(m0v), "v"(voff), "s"(rs), "s"(soff) : "memory");
}
__device__ __forceinline__ s16x4 tr_read(const char* p) {
  typedef __attribute__((ext_vector_type(4))) short v4i16_t;
  return __builtin_amdgcn_ds_read_tr16_b64_v4i16((LAS v4i16_t*)p);
}
__device__ __forceinline__ bf16x8 cat8(s16x4 a, s16x4 b) {
  return __builtin_shufflevector(a, b, 0, 1, 2, 3, 4, 5, 6, 7);
}
__device__ __forceinline__ float gelu_tanh(float x) {
  float u = x * (0.7978845608028654f + 0.035677408136300125f * x * x);
  float e = __builtin_amdgcn_exp2f(-2.885390081777927f * u);
  return x * __builtin_amdgcn_rcpf(1.0f + e);
}

#define SB() __builtin_amdgcn_sched_barrier(0)
template <int N> __device__ __forceinline__ void wait_v() { asm volatile("s_waitcnt vmcnt(%0)" :: "i"(N) : "memory"); }
template <int N> __device__ __forceinline__ void wait_l() { asm volatile("s_waitcnt lgkmcnt(%0)" :: "i"(N) : "memory"); }

constexpr int K1_SLOT = 24576, K1_NS = 6, K1_X_OFF = K1_NS * K1_SLOT;
constexpr int K1_LDS = K1_X_OFF + 8192;

__device__ __forceinline__ void pf_unit(const u16* p, unsigned unit, unsigned m0v, unsigned bytes = S_ * S_ * 2) {
  const rsrc_t rs = mk_rsrc(p, bytes);
  const unsigned voff = (threadIdx.x & 63) * 128;
  asm volatile("s_mov_b32 m0, %0\n\ts_nop 0\n\tbuffer_load_dword %1, %2, %3 offen lds"
               :: "s"(m0v), "v"(voff), "s"(rs), "s"(unit * 8192u) : "memory");
}

template <bool TR>
__device__ __forceinline__ void k1_body(char* smem, const u16* __restrict__ x, const u16* __restrict__ w0,
                                        const u16* __restrict__ w1, u16* __restrict__ outp, int rb, int ncol0,
                                        float sgn, const u16* pfsrc, unsigned pfu0) {
  const int tid = threadIdx.x;
  const int lane = tid & 63, wid = __builtin_amdgcn_readfirstlane(tid >> 6), wm = wid >> 2, wn = wid & 3;
  const int fr = lane & 15, fq = lane >> 4;
  const unsigned ldsb = (unsigned)(size_t)(LAS char*)smem;

  STAMP(0, 0); CSTAMP(0, 30);
  const unsigned rvoff = (lane >> 2) * (D_ * 2) + (((lane & 3) ^ ((-(lane >> 4)) & 3)) * 16);
  const rsrc_t rsx = mk_rsrc(x, S_ * D_ * 2);
  const unsigned asoff = (rb * 128 + 16 * wid) * (D_ * 2);
  unsigned bvoff, bsoff;
  rsrc_t rs0, rs1;
  if constexpr (!TR) {
    bvoff = rvoff;
    bsoff = (ncol0 + 16 * wid) * (D_ * 2);
    rs0 = mk_rsrc(w0, D_ * D_ * 2);
    rs1 = rs0;
  } else {
    const int f = (lane >> 4) | (((wid >> 1) & 1) << 2);
    bvoff = (lane >> 4) * (F_ * 2) + (((lane & 15) ^ (f << 1)) * 16);
    bsoff = 4 * wid * (F_ * 2) + ncol0 * 2;
    rs0 = mk_rsrc(w0, D_ * F_ * 2);
    rs1 = mk_rsrc(w1, D_ * F_ * 2);
  }
  const unsigned m0w = ldsb + wid * 1024;
#define K1_PIECE(t, i)                                                                                    \
  do {                                                                                                    \
    const unsigned m0_ = m0w + ((t) % K1_NS) * K1_SLOT + (i) * 8192;                                      \
    if ((i) == 0) dma16(m0_, rvoff, rsx, asoff + (t) * 64);                                               \
    else if constexpr (!TR) dma16(m0_, bvoff, rs0, bsoff + ((i) - 1) * (128 * D_ * 2) + (t) * 64);        \
    else dma16(m0_, bvoff, (i) == 2 ? rs1 : rs0, bsoff + (t) * (32 * F_ * 2));                            \
  } while (0)
#pragma unroll
  for (int t = 0; t < K1_NS - 1; ++t) { K1_PIECE(t, 0); K1_PIECE(t, 1); K1_PIECE(t, 2); }
  pf_unit(pfsrc, pfu0 + wid, ldsb + K1_X_OFF + 4096 + wid * 256);

  const int rsw = (fq ^ ((-(fr >> 2)) & 3)) << 4;
  int a_rd[4], b_rd[4];
#pragma unroll
  for (int i = 0; i < 4; ++i) a_rd[i] = (wm * 64 + ((i + wn) & 3) * 16 + fr) * 64 + rsw;
#pragma unroll
  for (int j = 0; j < 4; ++j) {
    const int ja = (j + 2 * wm) & 3;
    if constexpr (!TR) {
      b_rd[j] = 8192 + (wn * 64 + ja * 16 + fr) * 64 + rsw;
    } else {
      const int q = fr >> 2, pp = fr & 3, f = q | ((fq & 1) << 2);
      const int ch = 4 * wn + 2 * (ja & 1) + (pp >> 1);
      b_rd[j] = 8192 + (ja >> 1) * 8192 + (8 * fq + q) * 256 + ((ch ^ (f << 1)) << 4) + (pp & 1) * 8;
    }
  }

  f32x4 acc[4][4], acs[2], ars, agr;
#pragma unroll
  for (int j = 0; j < 4; ++j)
#pragma unroll
    for (int i = 0; i < 4; ++i) acc[j][i] = (f32x4){0.f, 0.f, 0.f, 0.f};
  acs[0] = acs[1] = ars = agr = (f32x4){0.f, 0.f, 0.f, 0.f};
  const bf16x8 ones = {0x3F80, 0x3F80, 0x3F80, 0x3F80, 0x3F80, 0x3F80, 0x3F80, 0x3F80};

  bf16x8 af[2][4], bfr[2][4];
#define K1_LDA(t, i) af[(t) & 1][i] = *(const bf16x8*)(smem + ((t) % K1_NS) * K1_SLOT + a_rd[i])
#define K1_LDB(t, j)                                                                                      \
  do {                                                                                                    \
    const char* p_ = smem + ((t) % K1_NS) * K1_SLOT + b_rd[j];                                            \
    if constexpr (!TR) bfr[(t) & 1][j] = *(const bf16x8*)p_;                                              \
    else bfr[(t) & 1][j] = cat8(tr_read(p_), tr_read(p_ + 4 * 256));                                      \
  } while (0)
#define MFMA(a, b, c) __builtin_amdgcn_mfma_f32_16x16x32_bf16(a, b, c, 0, 0, 0)
#define K1_MMA4(t, j)                                                                                     \
  _Pragma("unroll") for (int i_ = 0; i_ < 4; ++i_) acc[j][i_] = MFMA(bfr[(t) & 1][j], af[(t) & 1][i_], acc[j][i_])

  wait_v<3 * (K1_NS - 3) + 1>();
  BAR();
  STAMP(0, 2); CSTAMP(0, 28);
#pragma unroll
  for (int i = 0; i < 4; ++i) K1_LDA(0, i);
#pragma unroll
  for (int j = 0; j < 4; ++j) K1_LDB(0, j);

#pragma unroll
  for (int t = 0; t < 16; ++t) {
    K1_MMA4(t, 0);
    acs[0] = MFMA(bfr[t & 1][0], ones, acs[0]);
    if (t + 1 < 16) { K1_LDA(t + 1, 0); K1_LDA(t + 1, 1); K1_LDB(t + 1, 0); }
    if (t + K1_NS - 1 < 16) K1_PIECE(t + K1_NS - 1, 0);
    SB();
    K1_MMA4(t, 1);
    acs[1] = MFMA(bfr[t & 1][1], ones, acs[1]);
    if (t + 1 < 16) { K1_LDA(t + 1, 2); K1_LDA(t + 1, 3); K1_LDB(t + 1, 1); }
    if (t + K1_NS - 1 < 16) K1_PIECE(t + K1_NS - 1, 1);
    SB();
    K1_MMA4(t, 2);
    ars = MFMA(ones, af[t & 1][0], ars);
    if (t + 1 < 16) { K1_LDB(t + 1, 2); K1_LDB(t + 1, 3); }
    if (t + K1_NS - 1 < 16) K1_PIECE(t + K1_NS - 1, 2);
    SB();
    K1_MMA4(t, 3);
    agr = MFMA(af[t & 1][0], af[t & 1][0], agr);
    SB();
    if (t + 2 < 16) {
      const int last = t + K1_NS - 1 < 15 ? t + K1_NS - 1 : 15;
      const int n = 3 * (last - (t + 2)) + (t + 2 < K1_NS - 1 ? 1 : 0);
      switch (n) {
        case 10: wait_v<10>(); break;
        case 9: wait_v<9>(); break;
        case 6: wait_v<6>(); break;
        case 3: wait_v<3>(); break;
        default: wait_v<0>(); break;
      }
      BAR();
    }
    if (t == 3 || t == 7 || t == 11) STAMP(0, 3 + (t >> 2));
    if (t == 11) CSTAMP(0, 29);
  }

  STAMP(0, 10); CSTAMP(0, 31);
  float* xs = (float*)(smem + K1_X_OFF);
  {
    const float d = (fr & 2) ? ((fr & 1) ? agr[3] : agr[2]) : ((fr & 1) ? agr[1] : agr[0]);
    const float mean = ars[0] * (1.0f / D_);
    const float var = fmaxf(d * (1.0f / D_) - mean * mean, 0.f);
    if (fq == (fr >> 2)) {
      xs[wm * 64 + wn * 16 + fr] = mean;
      xs[128 + wm * 64 + wn * 16 + fr] = __builtin_amdgcn_rsqf(var + 1e-5f);
    }
    if (fr == 0) {
      *(f32x4*)(xs + 256 + wn * 64 + (2 * wm) * 16 + fq * 4) = acs[0];
      *(f32x4*)(xs + 256 + wn * 64 + (2 * wm + 1) * 16 + fq * 4) = acs[1];
    }
  }
  __syncthreads();
  f32x4 cs[4];
#pragma unroll
  for (int j = 0; j < 4; ++j) cs[j] = *(const f32x4*)(xs + 256 + wn * 64 + ((j + 2 * wm) & 3) * 16 + fq * 4);

#pragma unroll
  for (int i = 0; i < 4; ++i) {
    const int mrow = wm * 64 + ((i + wn) & 3) * 16 + fr;
    const float mean = xs[mrow], rstd = xs[128 + mrow];
    if constexpr (!TR) {
      u16* op = outp + (size_t)(rb * 128 + mrow) * D_ + ncol0 + wn * 64 + fq * 4;
      const float rs = rstd * sgn;
#pragma unroll
      for (int j = 0; j < 4; ++j) {
        const f32x4 v = (acc[j][i] - mean * cs[j]) * rs;
        u32x2 o;
        o[0] = pack_bf16(v[0], v[1]);
        o[1] = pack_bf16(v[2], v[3]);
        *(u32x2*)(op + ((j + 2 * wm) & 3) * 16) = o;
      }
    } else {
      const int fcol = ncol0 + wn * 32;
      const bool valid = fcol < F_;
      u16* op = outp + (size_t)(rb * 128 + mrow) * HLD + fcol + fq * 4;
#pragma unroll
      for (int jj = 0; jj < 2; ++jj) {
        u32x2 o = {0u, 0u};
        if (valid) {
          const f32x4 v0 = (acc[jj][i] - mean * cs[jj]) * rstd;
          const f32x4 v1 = (acc[2 + jj][i] - mean * cs[2 + jj]) * rstd;
          float v[4];
#pragma unroll
          for (int r = 0; r < 4; ++r) v[r] = gelu_tanh(wm ? v1[r] : v0[r]) * (wm ? v0[r] : v1[r]);
          o[0] = pack_bf16(v[0], v[1]);
          o[1] = pack_bf16(v[2], v[3]);
        }
        *(u32x2*)(op + jj * 16) = o;
      }
    }
  }
}

__global__ __launch_bounds__(512, 2) void k1_ln_gemm(const u16* __restrict__ x, const u16* __restrict__ wr,
                                                     const u16* __restrict__ wi, const u16* __restrict__ wg,
                                                     const u16* __restrict__ wu, u16* __restrict__ ab,
                                                     u16* __restrict__ h, unsigned* __restrict__ cnt,
                                                     const u16* __restrict__ cr, const u16* __restrict__ ci,
                                                     const u16* __restrict__ wd) {
  extern __shared__ __attribute__((aligned(16))) char smem[];
  const int bid = blockIdx.x;
  const u16* pfsrc = bid < 128 ? cr : ci;
  const unsigned pfu0 = (bid & 127) * 8;
  if (bid >= 240) {
    if (bid == 240 && threadIdx.x < 64) cnt[threadIdx.x * 32] = 0u;
    const unsigned w = __builtin_amdgcn_readfirstlane(threadIdx.x >> 6);
    const unsigned m0v = (unsigned)(size_t)(LAS char*)smem + w * 256;
    pf_unit(pfsrc, pfu0 + w, m0v);
    pf_unit(wd, (bid - 240) * 16 + w, m0v, F_ * D_ * 2);
    pf_unit(wd, (bid - 240) * 16 + 8 + w, m0v, F_ * D_ * 2);
    wait_v<0>();
    return;
  }
  const int id = (bid & 7) * 30 + (bid >> 3);
  const int ct = id >> 4, rb = id & 15;
  if (ct < 4) {
    k1_body<false>(smem, x, ct < 2 ? wr : wi, nullptr, ab + (ct < 2 ? 0 : (size_t)S_ * D_), rb, (ct & 1) * 256,
                   ct < 2 ? 1.0f : -1.0f, pfsrc, pfu0);
  } else {
    k1_body<true>(smem, x, wg, wu, h, rb, (ct - 4) * 128, 1.0f, pfsrc, pfu0);
  }
}

constexpr int K2_SLOT = 32768, K2_LDS = 4 * K2_SLOT;

struct K2Src { rsrc_t ra, rb; unsigned asoff, bsoff; };
__device__ __forceinline__ K2Src k2_src(int c, const u16* cr, const u16* ci, const u16* h, const u16* ab,
                                        const u16* wd, unsigned asoff0, unsigned bsoff0) {
  K2Src r;
  r.ra = mk_rsrc(c < 32 ? cr : (c < 64 ? ci : h), S_ * S_ * 2);
  r.asoff = asoff0 + (unsigned)(c & 31) * 128u;
  r.rb = mk_rsrc(c < 64 ? ab : wd, c < 64 ? 2 * S_ * D_ * 2 : F_ * D_ * 2);
  r.bsoff = bsoff0 + (unsigned)(c & 63) * (64u * 1024u);
  return r;
}
__device__ __forceinline__ void k2_piece(const K2Src& s, int i, unsigned m0b, unsigned avoff, unsigned bvoff) {
  if (i < 2) dma16(m0b + i * 8192, avoff, s.ra, s.asoff + i * (64u * 4096u));
  else dma16(m0b + i * 8192, bvoff, s.rb, s.bsoff + (i - 2) * (32u * 1024u));
}

__global__ __launch_bounds__(512, 2) void k2_gemm(const u16* __restrict__ cr, const u16* __restrict__ ci,
                                                  const u16* __restrict__ h, const u16* __restrict__ ab,
                                                  const u16* __restrict__ wd, const u16* __restrict__ x,
                                                  float* __restrict__ slab, unsigned* __restrict__ cnt,
                                                  u16* __restrict__ out) {
  extern __shared__ __attribute__((aligned(16))) char smem[];
  const int tid = threadIdx.x, lane = tid & 63, wid = __builtin_amdgcn_readfirstlane(tid >> 6);
  const int kg = wid >> 2, wt = (wid >> 1) & 1, we = wid & 1;
  const int fr = lane & 15, fq = lane >> 4;
  const unsigned ldsb = (unsigned)(size_t)(LAS char*)smem;
  const int bid = blockIdx.x;
  const int xcd = bid & 7, loc = bid >> 3;
  const int p = xcd >> 1;
  const int trow = (xcd & 1) * 8 + (loc >> 2), tcol = loc & 3;
  const int tile = trow * 4 + tcol;
  const bool full = p < 2;
  const int c0 = p < 2 ? p * 22 : 44 + (p - 2) * 21 - 1;

  STAMP(1, 0); CSTAMP(1, 30);
  const unsigned avoff = (unsigned)(tid >> 3) * 4096u + (((tid & 7) ^ ((tid >> 4) & 7)) * 16);
  const int bk = tid >> 4;
  const int bf_ = (bk & 3) | (((bk >> 3) & 1) << 2);
  const unsigned bvoff = (unsigned)bk * 1024u + (((tid & 15) ^ (bf_ << 1)) * 16);
  const unsigned m0w = ldsb + wid * 1024;
  const unsigned asoff0 = (unsigned)trow * (128u * 4096u), bsoff0 = (unsigned)tcol * 256u;
#define K2SRC(pos) k2_src(c0 + (pos), cr, ci, h, ab, wd, asoff0, bsoff0)
#define stage(pos, slot)                                                                  \
  do {                                                                                    \
    const K2Src sr_ = K2SRC(pos);                                                         \
    _Pragma("unroll") for (int i_ = 0; i_ < 4; ++i_) k2_piece(sr_, i_, m0w + (slot) * K2_SLOT, avoff, bvoff); \
  } while (0)

  const int asw = (fr >> 1) & 7;
  int a_rd[4];
#pragma unroll
  for (int i = 0; i < 4; ++i) a_rd[i] = (wt * 64 + ((i + p) & 3) * 16 + fr) * 128 + (((fq + 4 * kg) ^ asw) << 4);
  int b_rd[4];
  {
    const int q = fr >> 2, pp = fr & 3, f = q | ((fq & 1) << 2);
#pragma unroll
    for (int j = 0; j < 4; ++j) {
      const int ch = 8 * we + 2 * j + (pp >> 1);
      b_rd[j] = 16384 + (32 * kg + 8 * fq + q) * 256 + ((ch ^ (f << 1)) << 4) + (pp & 1) * 8;
    }
  }

  f32x4 acc[4][4];
#pragma unroll
  for (int j = 0; j < 4; ++j)
#pragma unroll
    for (int i = 0; i < 4; ++i) acc[j][i] = (f32x4){0.f, 0.f, 0.f, 0.f};

  bf16x8 af0[4], bf0[4], af1[4], bf1[4];
#define LDA(dst, i, slot) dst[i] = *(const bf16x8*)(smem + (slot) * K2_SLOT + a_rd[i])
#define LDB(dst, j, slot)                                                             \
  do {                                                                                \
    const char* pb_ = smem + (slot) * K2_SLOT + b_rd[j];                              \
    dst[j] = cat8(tr_read(pb_), tr_read(pb_ + 4 * 256));                              \
  } while (0)
#define MMA4(j, af, bfr)                                                              \
  _Pragma("unroll") for (int i_ = 0; i_ < 4; ++i_)                                    \
      acc[j][i_] = __builtin_amdgcn_mfma_f32_16x16x32_bf16(bfr[j], af[i_], acc[j][i_], 0, 0, 0)
#define PIECE(DO, i, slot) do { if (DO) k2_piece(sr, i, m0w + (slot) * K2_SLOT, avoff, bvoff); } while (0)
#define RDALL(af, bfr, slot)                                                          \
  do {                                                                                \
    LDA(af, 0, slot); LDA(af, 1, slot); LDA(af, 2, slot); LDA(af, 3, slot);           \
    LDB(bfr, 0, slot); LDB(bfr, 1, slot); LDB(bfr, 2, slot); LDB(bfr, 3, slot);       \
  } while (0)
#define K2_POS(j, slot, afc, bfc, afn, bfn, DO, RD)                                   \
  do {                                                                                \
    const K2Src sr = K2SRC((j) + 3);                                                  \
    MMA4(0, afc, bfc);                                                                \
    if (RD) { LDA(afn, 0, ((slot) + 1) & 3); LDA(afn, 1, ((slot) + 1) & 3); LDB(bfn, 0, ((slot) + 1) & 3); } \
    PIECE(DO, 0, ((slot) + 3) & 3); SB();                                             \
    MMA4(1, afc, bfc);                                                                \
    if (RD) { LDA(afn, 2, ((slot) + 1) & 3); LDA(afn, 3, ((slot) + 1) & 3); LDB(bfn, 1, ((slot) + 1) & 3); } \
    PIECE(DO, 1, ((slot) + 3) & 3); SB();                                             \
    MMA4(2, afc, bfc);                                                                \
    if (RD) { LDB(bfn, 2, ((slot) + 1) & 3); LDB(bfn, 3, ((slot) + 1) & 3); }         \
    PIECE(DO, 2, ((slot) + 3) & 3); SB();                                             \
    MMA4(3, afc, bfc);                                                                \
    PIECE(DO, 3, ((slot) + 3) & 3); SB();                                             \
  } while (0)

  if (full) {
    stage(0, 0);
    stage(1, 1);
    stage(2, 2);
    WAIT_V(4); BAR();
    RDALL(af0, bf0, 0);
    K2_POS(0, 0, af0, bf0, af1, bf1, true, true);
    WAIT_V(4); BAR();
  } else {
    stage(1, 1);
    stage(2, 2);
    stage(3, 3);
    WAIT_V(4); BAR();
    RDALL(af1, bf1, 1);
  }
  STAMP(1, 1);
  for (int j = 1; j < 17; j += 4) {
    STAMP(1, 2 + (j >> 2)); if (j == 1) CSTAMP(1, 28);
    K2_POS(j, 1, af1, bf1, af0, bf0, true, true);     WAIT_V(4); BAR();
    K2_POS(j + 1, 2, af0, bf0, af1, bf1, true, true); WAIT_V(4); BAR();
    K2_POS(j + 2, 3, af1, bf1, af0, bf0, true, true); WAIT_V(4); BAR();
    K2_POS(j + 3, 0, af0, bf0, af1, bf1, true, true); WAIT_V(4); BAR();
  }
  STAMP(1, 6); CSTAMP(1, 29);
  K2_POS(17, 1, af1, bf1, af0, bf0, true, true);  WAIT_V(4); BAR();
  K2_POS(18, 2, af0, bf0, af1, bf1, true, true);  WAIT_V(4); BAR();
  K2_POS(19, 3, af1, bf1, af0, bf0, false, true); WAIT_V(0); BAR();
  K2_POS(20, 0, af0, bf0, af1, bf1, false, true);
  K2_POS(21, 1, af1, bf1, af0, bf0, false, false);

  STAMP(1, 7); CSTAMP(1, 31);
  {
    char* xb = smem + 2 * K2_SLOT + (wid & 3) * 16384 + lane * 16;
#pragma unroll
    for (int jj = 0; jj < 2; ++jj)
#pragma unroll
      for (int i = 0; i < 4; ++i) {
        const f32x4 v = kg ? acc[jj][i] : acc[2 + jj][i];
        *(f32x4*)(xb + kg * 8192 + (jj * 4 + i) * 1024) = v;
      }
    __syncthreads();
#pragma unroll
    for (int jj = 0; jj < 2; ++jj)
#pragma unroll
      for (int i = 0; i < 4; ++i) {
        const f32x4 o = *(const f32x4*)(xb + (kg ^ 1) * 8192 + (jj * 4 + i) * 1024);
        const f32x4 mine = kg ? acc[2 + jj][i] : acc[jj][i];
        acc[jj][i] = mine + o;
      }
  }
  const rsrc_t rs = mk_rsrc(slab, 64 * 16 * 4096 * 4);
  const unsigned lane_off = (unsigned)(wid * 2 * 64 + lane) * 16;
#pragma unroll
  for (int i = 1; i < 4; ++i) {
    const unsigned dstq = (i + p) & 3;
    const unsigned off = ((tile * 4 + dstq) * 4 + p) * 16384u + lane_off;
#pragma unroll
    for (int jj = 0; jj < 2; ++jj)
      __builtin_amdgcn_raw_buffer_store_b128(__builtin_bit_cast(u32x4, acc[jj][i]), rs, off + jj * 1024, 0, 16);
  }
  const int trw = trow * 128 + wt * 64 + p * 16 + fr;
  const int ecol = tcol * 128 + we * 64 + kg * 32 + fq * 4;
  u32x2 xr[2];
#pragma unroll
  for (int jj = 0; jj < 2; ++jj) xr[jj] = *(const u32x2*)(x + (size_t)trw * D_ + ecol + jj * 16);
  asm volatile("s_waitcnt vmcnt(0)" ::: "memory");
  __syncthreads();
  STAMP(1, 8);
  GAS unsigned* cw = (GAS unsigned*)(cnt + tile * 32);
  if (wid == 0) {
    if (lane == 0) __hip_atomic_fetch_add(cw, 1u, __ATOMIC_RELAXED, __HIP_MEMORY_SCOPE_AGENT);
    for (unsigned spins = 0; spins < (1u << 20); ++spins) {
      unsigned v = __hip_atomic_load(cw, __ATOMIC_RELAXED, __HIP_MEMORY_SCOPE_AGENT);
      if ((unsigned)__builtin_amdgcn_readfirstlane(v) >= 4u) break;
      __builtin_amdgcn_s_sleep(1);
    }
  }
  __syncthreads();
  __builtin_amdgcn_fence(__ATOMIC_ACQUIRE, "wavefront");
  STAMP(1, 9);

  u32x4 pv[3][2];
#pragma unroll
  for (int s = 1; s < 4; ++s) {
    const unsigned srcq = (s + p) & 3;
    const unsigned off = ((tile * 4 + p) * 4 + srcq) * 16384u + lane_off;
#pragma unroll
    for (int jj = 0; jj < 2; ++jj) pv[s - 1][jj] = __builtin_amdgcn_raw_buffer_load_b128(rs, off + jj * 1024, 0, 16);
  }
#pragma unroll
  for (int jj = 0; jj < 2; ++jj) {
    f32x4 v = acc[jj][0];
#pragma unroll
    for (int s = 0; s < 3; ++s) v += __builtin_bit_cast(f32x4, pv[s][jj]);
    u32x2 o;
    o[0] = pack_bf16(v[0] + bf_lo(xr[jj][0]), v[1] + bf_hi(xr[jj][0]));
    o[1] = pack_bf16(v[2] + bf_lo(xr[jj][1]), v[3] + bf_hi(xr[jj][1]));
    *(u32x2*)(out + (size_t)trw * D_ + ecol + jj * 16) = o;
  }
}

extern "C" void kernel_launch(void* const* d_in, const int* in_sizes, int n_in, void* d_out, int out_size,
                              void* d_ws, size_t ws_size, hipStream_t stream) {
  const u16* x = (const u16*)d_in[0];
  const u16* cr = (const u16*)d_in[1];
  const u16* ci = (const u16*)d_in[2];
  const u16* wr = (const u16*)d_in[3];
  const u16* wi = (const u16*)d_in[4];
  const u16* wg = (const u16*)d_in[6];
  const u16* wu = (const u16*)d_in[7];
  const u16* wd = (const u16*)d_in[8];
  char* ws = (char*)d_ws;
  u16* ab = (u16*)(ws + WS_AB);
  u16* h = (u16*)(ws + WS_H);
  float* slab = (float*)(ws + WS_SLAB);
  unsigned* cnt = (unsigned*)(ws + WS_CNT);
  (void)hipFuncSetAttribute((const void*)k1_ln_gemm, hipFuncAttributeMaxDynamicSharedMemorySize, K1_LDS);
  (void)hipFuncSetAttribute((const void*)k2_gemm, hipFuncAttributeMaxDynamicSharedMemorySize, K2_LDS);
  k1_ln_gemm<<<256, 512, K1_LDS, stream>>>(x, wr, wi, wg, wu, ab, h, cnt, cr, ci, wd);
  k2_gemm<<<256, 512, K2_LDS, stream>>>(cr, ci, h, ab, wd, x, slab, cnt, (u16*)d_out);
}
```

```cpp
#include <hip/hip_runtime.h>
#include <hip/hip_bf16.h>
#include <stdint.h>

typedef __attribute__((ext_vector_type(8))) short bf16x8;
typedef __attribute__((ext_vector_type(4))) short s16x4;
typedef __attribute__((ext_vector_type(4))) float f32x4;
typedef __attribute__((ext_vector_type(2))) float f32x2;
typedef __attribute__((ext_vector_type(4))) unsigned u32x4;
typedef __attribute__((ext_vector_type(2))) unsigned u32x2;
typedef __attribute__((ext_vector_type(2))) __bf16 bf2_t;
typedef unsigned short u16;
typedef __amdgpu_buffer_rsrc_t rsrc_t;

#define LAS __attribute__((address_space(3)))
#define GAS __attribute__((address_space(1)))

constexpr int S_ = 2048, D_ = 512, F_ = 1376;
constexpr int HLD = 2048;

constexpr size_t WS_AB = 0;
constexpr size_t WS_H = 4u << 20;
constexpr size_t WS_SLAB = 16u << 20;
constexpr size_t WS_CNT = 48u << 20;

#define WAIT_V(n) asm volatile("s_waitcnt vmcnt(" #n ")" ::: "memory")
#define WAIT_L0() asm volatile("s_waitcnt lgkmcnt(0)" ::: "memory")
#define BAR() __builtin_amdgcn_s_barrier()
#define STAMP(k, i) do {} while (0)
#define CSTAMP(k, i) do {} while (0)

__device__ __forceinline__ unsigned pack_bf16(float a, float b) {
  f32x2 v = {a, b};
  bf2_t r = __builtin_convertvector(v, bf2_t);
  return __builtin_bit_cast(unsigned, r);
}
__device__ __forceinline__ float bf_lo(unsigned u) { return __builtin_bit_cast(float, u << 16); }
__device__ __forceinline__ float bf_hi(unsigned u) { return __builtin_bit_cast(float, u & 0xffff0000u); }

__device__ __forceinline__ rsrc_t mk_rsrc(const void* p, unsigned bytes) {
  return __builtin_amdgcn_make_buffer_rsrc((void*)p, (short)0, (int)bytes, 0x00020000);
}
__device__ __forceinline__ void dma16(unsigned m0v, unsigned voff, rsrc_t rs, unsigned soff) {
  asm volatile("s_mov_b32 m0, %0\n\ts_nop 0\n\tbuffer_load_dwordx4 %1, %2, %3 offen lds"
               :: "s"(m0v), "v"(voff), "s"(rs), "s"(soff) : "memory");
}
__device__ __forceinline__ s16x4 tr_read(const char* p) {
  typedef __attribute__((ext_vector_type(4))) short v4i16_t;
  return __builtin_amdgcn_ds_read_tr16_b64_v4i16((LAS v4i16_t*)p);
}
__device__ __forceinline__ bf16x8 cat8(s16x4 a, s16x4 b) {
  return __builtin_shufflevector(a, b, 0, 1, 2, 3, 4, 5, 6, 7);
}
__device__ __forceinline__ float gelu_tanh(float x) {
  float u = x * (0.7978845608028654f + 0.035677408136300125f * x * x);
  float e = __builtin_amdgcn_exp2f(-2.885390081777927f * u);
  return x * __builtin_amdgcn_rcpf(1.0f + e);
}

#define SB() __builtin_amdgcn_sched_barrier(0)
template <int N> __device__ __forceinline__ void wait_v() { asm volatile("s_waitcnt vmcnt(%0)" :: "i"(N) : "memory"); }
template <int N> __device__ __forceinline__ void wait_l() { asm volatile("s_waitcnt lgkmcnt(%0)" :: "i"(N) : "memory"); }

constexpr int K1_NS = 6, K1_B_OFF = K1_NS * 8192, K1_X_OFF = K1_NS * 24576;
#define K1_PF_POS 13
constexpr int K1_LDS = K1_X_OFF + 8192;

__device__ __forceinline__ void pf_load(unsigned m0v, unsigned voff, rsrc_t rs, unsigned soff) {
  asm volatile("s_mov_b32 m0, %0\n\ts_nop 0\n\tbuffer_load_dword %1, %2, %3 offen lds"
               :: "s"(m0v), "v"(voff), "s"(rs), "s"(soff) : "memory");
}
__device__ __forceinline__ void k1_pf_far(unsigned m0v, int bid, int w, const u16* cr, const u16* ci) {
  pf_load(m0v, (threadIdx.x & 63) * 128, mk_rsrc(bid < 128 ? cr : ci, S_ * S_ * 2), ((bid & 127) * 8 + w) * 8192u);
}

template <bool TR>
__device__ __forceinline__ void k1_body(char* smem, const u16* __restrict__ x, const u16* __restrict__ w0,
                                        const u16* __restrict__ w1, u16* __restrict__ outp, int rb, int ncol0,
                                        float sgn, const u16* cr, const u16* ci, const u16* wd) {
  const int tid = threadIdx.x;
  const int lane = tid & 63, wid = __builtin_amdgcn_readfirstlane(tid >> 6), wm = wid >> 2, wn = wid & 3;
  int fr = lane & 15, fq = lane >> 4;
  const unsigned ldsb = (unsigned)(size_t)(LAS char*)smem;

  STAMP(0, 0); CSTAMP(0, 30);
  const unsigned rvoff = (lane >> 2) * (D_ * 2) + (((lane & 3) ^ ((-(lane >> 4)) & 3)) * 16);
  const rsrc_t rsx = mk_rsrc(x, S_ * D_ * 2);
  const unsigned asoff = (rb * 128 + 16 * wid) * (D_ * 2);
  unsigned bvoff, bsoff;
  rsrc_t rs0, rs1;
  if constexpr (!TR) {
    bvoff = rvoff;
    bsoff = (ncol0 + 16 * wid) * (D_ * 2);
    rs0 = mk_rsrc(w0, D_ * D_ * 2);
    rs1 = rs0;
  } else {
    const int f = (lane >> 4) | (((wid >> 1) & 1) << 2);
    bvoff = (lane >> 4) * (F_ * 2) + (((lane & 15) ^ (f << 1)) * 16);
    bsoff = 4 * wid * (F_ * 2) + ncol0 * 2;
    rs0 = mk_rsrc(w0, D_ * F_ * 2);
    rs1 = mk_rsrc(w1, D_ * F_ * 2);
  }
  const unsigned m0w = ldsb + wid * 1024;
#define K1_ROT(rb, ncol0) (((rb) + ((ncol0) >> 7) * 3) & 15)
  const int rot = K1_ROT(rb, ncol0);
#define K1_PIECE(t, i)                                                                                    \
  do {                                                                                                    \
    const unsigned m0_ = m0w + ((i) == 0 ? ((t) % K1_NS) * 8192 : K1_B_OFF + ((t) % K1_NS) * 16384 + ((i) - 1) * 8192); \
    const int tr_ = ((t) + rot) & 15;                                                                     \
    if ((i) == 0) dma16(m0_, rvoff, rsx, asoff + tr_ * 64);                                               \
    else if constexpr (!TR) dma16(m0_, bvoff, rs0, bsoff + ((i) - 1) * (128 * D_ * 2) + tr_ * 64);        \
    else dma16(m0_, bvoff, (i) == 2 ? rs1 : rs0, bsoff + tr_ * (32 * F_ * 2));                            \
  } while (0)
#pragma unroll
  for (int t = 0; t < K1_NS - 1; ++t) { K1_PIECE(t, 0); K1_PIECE(t, 1); K1_PIECE(t, 2); }

  const int rsw = (fq ^ ((-(fr >> 2)) & 3)) << 4;
  int a_rd[4], b_rd[4];
#pragma unroll
  for (int i = 0; i < 4; ++i) a_rd[i] = (wm * 64 + ((i + wn) & 3) * 16 + fr) * 64 + rsw;
#pragma unroll
  for (int j = 0; j < 4; ++j) {
    const int ja = (j + 2 * wm) & 3;
    if constexpr (!TR) {
      b_rd[j] = K1_B_OFF + (wn * 64 + ja * 16 + fr) * 64 + rsw;
    } else {
      const int q = fr >> 2, pp = fr & 3, f = q | ((fq & 1) << 2);
      const int ch = 4 * wn + 2 * (ja & 1) + (pp >> 1);
      b_rd[j] = K1_B_OFF + (ja >> 1) * 8192 + (8 * fq + q) * 256 + ((ch ^ (f << 1)) << 4) + (pp & 1) * 8;
    }
  }

  f32x4 acc[4][4], acs[2], ars, agr;
#pragma unroll
  for (int j = 0; j < 4; ++j)
#pragma unroll
    for (int i = 0; i < 4; ++i) acc[j][i] = (f32x4){0.f, 0.f, 0.f, 0.f};
  acs[0] = acs[1] = ars = agr = (f32x4){0.f, 0.f, 0.f, 0.f};
  const bf16x8 ones = {0x3F80, 0x3F80, 0x3F80, 0x3F80, 0x3F80, 0x3F80, 0x3F80, 0x3F80};

  bf16x8 af[2][4], bfr[2][4];
#define K1_LDA(t, i) af[(t) & 1][i] = *(const bf16x8*)(smem + ((t) % K1_NS) * 8192 + a_rd[i])
#define K1_LDB(t, j)                                                                                      \
  do {                                                                                                    \
    const char* p_ = smem + ((t) % K1_NS) * 16384 + b_rd[j];                                              \
    if constexpr (!TR) bfr[(t) & 1][j] = *(const bf16x8*)p_;                                              \
    else bfr[(t) & 1][j] = cat8(tr_read(p_), tr_read(p_ + 4 * 256));                                      \
  } while (0)
#define MFMA(a, b, c) __builtin_amdgcn_mfma_f32_16x16x32_bf16(a, b, c, 0, 0, 0)
#define K1_MMA4(t, j)                                                                                     \
  _Pragma("unroll") for (int i_ = 0; i_ < 4; ++i_) acc[j][i_] = MFMA(bfr[(t) & 1][j], af[(t) & 1][i_], acc[j][i_])

  wait_v<3 * (K1_NS - 3)>();
  BAR();
  STAMP(0, 2); CSTAMP(0, 28);
#pragma unroll
  for (int i = 0; i < 4; ++i) K1_LDA(0, i);
#pragma unroll
  for (int j = 0; j < 4; ++j) K1_LDB(0, j);

#pragma unroll
  for (int t = 0; t < 16; ++t) {
    K1_MMA4(t, 0);
    acs[0] = MFMA(bfr[t & 1][0], ones, acs[0]);
    if (t + 1 < 16) { K1_LDA(t + 1, 0); K1_LDA(t + 1, 1); K1_LDB(t + 1, 0); }
    if (t + K1_NS - 1 < 16) K1_PIECE(t + K1_NS - 1, 0);
    SB();
    K1_MMA4(t, 1);
    acs[1] = MFMA(bfr[t & 1][1], ones, acs[1]);
    if (t + 1 < 16) { K1_LDA(t + 1, 2); K1_LDA(t + 1, 3); K1_LDB(t + 1, 1); }
    if (t + K1_NS - 1 < 16) K1_PIECE(t + K1_NS - 1, 1);
    SB();
    K1_MMA4(t, 2);
    ars = MFMA(ones, af[t & 1][0], ars);
    if (t + 1 < 16) { K1_LDB(t + 1, 2); K1_LDB(t + 1, 3); }
    if (t + K1_NS - 1 < 16) K1_PIECE(t + K1_NS - 1, 2);
    SB();
    K1_MMA4(t, 3);
    agr = MFMA(af[t & 1][0], af[t & 1][0], agr);
    if (t == K1_PF_POS) k1_pf_far(ldsb + K1_X_OFF + 4096 + wid * 256, blockIdx.x, wid, cr, ci);
    SB();
    if (t + 2 < 16) {
      const int last = t + K1_NS - 1 < 15 ? t + K1_NS - 1 : 15;
      const int n = 3 * (last - (t + 2)) + ((t >= K1_PF_POS && t + 2 <= K1_PF_POS + K1_NS - 1) ? 1 : 0);
      switch (n) {
        case 10: wait_v<10>(); break;
        case 9: wait_v<9>(); break;
        case 7: wait_v<7>(); break;
        case 6: wait_v<6>(); break;
        case 4: wait_v<4>(); break;
        case 3: wait_v<3>(); break;
        case 1: wait_v<1>(); break;
        default: wait_v<0>(); break;
      }
      BAR();
    }
    if (t == 3 || t == 7 || t == 11) STAMP(0, 3 + (t >> 2));
    if (t == 11) CSTAMP(0, 29);
  }

  STAMP(0, 10); CSTAMP(0, 31);
  {
    int tz = threadIdx.x;
    asm volatile("" : "+v"(tz));
    fr = tz & 15; fq = (tz >> 4) & 3;
  }
  float* xs = (float*)(smem + K1_X_OFF);
  {
    const float d = (fr & 2) ? ((fr & 1) ? agr[3] : agr[2]) : ((fr & 1) ? agr[1] : agr[0]);
    const float mean = ars[0] * (1.0f / D_);
    const float var = fmaxf(d * (1.0f / D_) - mean * mean, 0.f);
    if (fq == (fr >> 2)) {
      xs[wm * 64 + wn * 16 + fr] = mean;
      xs[128 + wm * 64 + wn * 16 + fr] = __builtin_amdgcn_rsqf(var + 1e-5f);
    }
    if (fr == 0) {
      *(f32x4*)(xs + 256 + wn * 64 + (2 * wm) * 16 + fq * 4) = acs[0];
      *(f32x4*)(xs + 256 + wn * 64 + (2 * wm + 1) * 16 + fq * 4) = acs[1];
    }
  }
  __syncthreads();
  f32x4 cs[4];
#pragma unroll
  for (int j = 0; j < 4; ++j) cs[j] = *(const f32x4*)(xs + 256 + wn * 64 + ((j + 2 * wm) & 3) * 16 + fq * 4);

#pragma unroll
  for (int i = 0; i < 4; ++i) {
    const int mrow = wm * 64 + ((i + wn) & 3) * 16 + fr;
    const float mean = xs[mrow], rstd = xs[128 + mrow];
    if constexpr (!TR) {
      u16* op = outp + (size_t)(rb * 128 + mrow) * D_ + ncol0 + wn * 64 + fq * 4;
      const float rs = rstd * sgn;
#pragma unroll
      for (int j = 0; j < 4; ++j) {
        const f32x4 v = (acc[j][i] - mean * cs[j]) * rs;
        u32x2 o;
        o[0] = pack_bf16(v[0], v[1]);
        o[1] = pack_bf16(v[2], v[3]);
        *(u32x2*)(op + ((j + 2 * wm) & 3) * 16) = o;
      }
    } else {
      const int fcol = ncol0 + wn * 32;
      const bool valid = fcol < F_;
      u16* op = outp + (size_t)(rb * 128 + mrow) * HLD + fcol + fq * 4;
#pragma unroll
      for (int jj = 0; jj < 2; ++jj) {
        u32x2 o = {0u, 0u};
        if (valid) {
          const f32x4 v0 = (acc[jj][i] - mean * cs[jj]) * rstd;
          const f32x4 v1 = (acc[2 + jj][i] - mean * cs[2 + jj]) * rstd;
          float v[4];
#pragma unroll
          for (int r = 0; r < 4; ++r) v[r] = gelu_tanh(wm ? v1[r] : v0[r]) * (wm ? v0[r] : v1[r]);
          o[0] = pack_bf16(v[0], v[1]);
          o[1] = pack_bf16(v[2], v[3]);
        }
        *(u32x2*)(op + jj * 16) = o;
      }
    }
  }
}

__global__ __launch_bounds__(512, 2) void k1_ln_gemm(const u16* __restrict__ x, const u16* __restrict__ wr,
                                                     const u16* __restrict__ wi, const u16* __restrict__ wg,
                                                     const u16* __restrict__ wu, u16* __restrict__ ab,
                                                     u16* __restrict__ h, unsigned* __restrict__ cnt,
                                                     const u16* __restrict__ cr, const u16* __restrict__ ci,
                                                     const u16* __restrict__ wd) {
  extern __shared__ __attribute__((aligned(16))) char smem[];
  const int bid = blockIdx.x;
  if (bid >= 240) {
    if (bid == 240 && threadIdx.x < 64) cnt[threadIdx.x * 32] = 0u;
    const int w = __builtin_amdgcn_readfirstlane(threadIdx.x >> 6);
    const unsigned m0v = (unsigned)(size_t)(LAS char*)smem + w * 256;
    const rsrc_t rw = mk_rsrc(wd, F_ * D_ * 2);
    pf_load(m0v, (threadIdx.x & 63) * 128, rw, ((bid - 240) * 16 + w) * 8192u);
    pf_load(m0v, (threadIdx.x & 63) * 128, rw, ((bid - 240) * 16 + 8 + w) * 8192u);
    k1_pf_far(m0v, bid, w, cr, ci);
    wait_v<0>();
    return;
  }
  const int id = (bid & 7) * 30 + (bid >> 3);
  const int ct = id >> 4, rb = id & 15;
  if (ct < 4) {
    k1_body<false>(smem, x, ct < 2 ? wr : wi, nullptr, ab + (ct < 2 ? 0 : (size_t)S_ * D_), rb, (ct & 1) * 256,
                   ct < 2 ? 1.0f : -1.0f, cr, ci, wd);
  } else {
    k1_body<true>(smem, x, wg, wu, h, rb, (ct - 4) * 128, 1.0f, cr, ci, wd);
  }
}

constexpr int K2_B_OFF = 6 * 16384, K2_LDS = 10 * 16384;

__device__ __forceinline__ void wait_v_rt(int n) {
  if (n >= 12) wait_v<12>();
  else if (n >= 8) wait_v<8>();
  else if (n >= 4) wait_v<4>();
  else wait_v<0>();
}

__global__ __launch_bounds__(512, 2) void k2_gemm(const u16* __restrict__ cr, const u16* __restrict__ ci,
                                                  const u16* __restrict__ h, const u16* __restrict__ ab,
                                                  const u16* __restrict__ wd, const u16* __restrict__ x,
                                                  float* __restrict__ slab, unsigned* __restrict__ cnt,
                                                  u16* __restrict__ out) {
  extern __shared__ __attribute__((aligned(16))) char smem[];
  const int tid = threadIdx.x, lane = tid & 63, wid = __builtin_amdgcn_readfirstlane(tid >> 6);
  const int kg = wid >> 2, wt = (wid >> 1) & 1, we = wid & 1;
  const int fr = lane & 15, fq = lane >> 4;
  const unsigned ldsb = (unsigned)(size_t)(LAS char*)smem;
  const int bid = blockIdx.x;
  const int xcd = bid & 7, loc = bid >> 3;
  const int p = xcd >> 1;
  const int trow = (xcd & 1) * 8 + (loc >> 2), tcol = loc & 3;
  const int tile = trow * 4 + tcol;
  const bool full = p < 2;
  const int cb = p < 2 ? p * 22 : 44 + (p - 2) * 21;
  const int nch = full ? 22 : 21;
#define K2_ROT(trow) (((trow) * 5) % 21)
  const int rot = K2_ROT(trow) - (full ? 0 : 1);

  STAMP(1, 0); CSTAMP(1, 30);
  const int w4 = wid & 3;
  unsigned voff, soff0, sstep, m0w;
  if (kg == 0) {
    voff = (lane >> 3) * 4096u + (((lane & 7) ^ ((((w4 & 1) << 2) + (lane >> 4)) & 7)) * 16);
    soff0 = (unsigned)(trow * 128 + w4 * 8) * 4096u;
    sstep = 32u * 4096u;
    m0w = ldsb + w4 * 1024;
  } else {
    const int f = (lane >> 4) | (((w4 >> 1) & 1) << 2);
    voff = (lane >> 4) * 1024u + (((lane & 15) ^ (f << 1)) * 16);
    soff0 = (unsigned)tcol * 256u + w4 * 4096u;
    sstep = 16u * 1024u;
    m0w = ldsb + K2_B_OFF + w4 * 1024;
  }
  const int depth = kg ? 3 : 5;
#define K2_SRC(pos)                                                                                          \
  const int cr_ = (pos) + rot;                                                                               \
  const int c_ = cb + (cr_ >= nch ? cr_ - nch : cr_);                                                        \
  const rsrc_t rs_ = kg ? mk_rsrc(c_ < 64 ? ab : wd, c_ < 64 ? 2 * S_ * D_ * 2 : F_ * D_ * 2)                \
                        : mk_rsrc(c_ < 32 ? cr : (c_ < 64 ? ci : h), S_ * S_ * 2);                           \
  const unsigned so_ = soff0 + (kg ? (unsigned)(c_ & 63) * 65536u : (unsigned)(c_ & 31) * 128u);

  const int asw = (fr >> 1) & 7;
  int a_rd[4];
#pragma unroll
  for (int i = 0; i < 4; ++i) a_rd[i] = (wt * 64 + ((i + p) & 3) * 16 + fr) * 128 + (((fq + 4 * kg) ^ asw) << 4);
  int b_rd[4];
  {
    const int q = fr >> 2, pp = fr & 3, f = q | ((fq & 1) << 2);
#pragma unroll
    for (int j = 0; j < 4; ++j) {
      const int ch = 8 * we + 2 * j + (pp >> 1);
      b_rd[j] = K2_B_OFF + (32 * kg + 8 * fq + q) * 256 + ((ch ^ (f << 1)) << 4) + (pp & 1) * 8;
    }
  }

  f32x4 acc[4][4];
#pragma unroll
  for (int j = 0; j < 4; ++j)
#pragma unroll
    for (int i = 0; i < 4; ++i) acc[j][i] = (f32x4){0.f, 0.f, 0.f, 0.f};

  bf16x8 af0[4], bf0[4], af1[4], bf1[4];
#define LDA(dst, i, aoff) dst[i] = *(const bf16x8*)(smem + (aoff) + a_rd[i])
#define LDB(dst, j, slot)                                                             \
  do {                                                                                \
    const char* pb_ = smem + (slot) * 16384 + b_rd[j];                                \
    dst[j] = cat8(tr_read(pb_), tr_read(pb_ + 4 * 256));                              \
  } while (0)
#define MMA4(j, af, bfr)                                                              \
  _Pragma("unroll") for (int i_ = 0; i_ < 4; ++i_) acc[j][i_] = MFMA(bfr[j], af[i_], acc[j][i_])
#define PIECE(i) do { if (go_) dma16(md_ + (i) * 4096, voff, rs_, so_ + (i) * sstep); } while (0)
#define K2_POS(j, bslot, afc, bfc, afn, bfn, RD)                                      \
  do {                                                                                \
    K2_SRC((j) + depth);                                                              \
    const bool go_ = (j) + depth < 22;                                                \
    const unsigned md_ = m0w + (kg ? (((bslot) + 3) & 3) * 16384 : asm1);             \
    MMA4(0, afc, bfc);                                                                \
    if (RD) { LDA(afn, 0, as1); LDA(afn, 1, as1); LDB(bfn, 0, ((bslot) + 1) & 3); }   \
    PIECE(0); SB();                                                                   \
    MMA4(1, afc, bfc);                                                                \
    if (RD) { LDA(afn, 2, as1); LDA(afn, 3, as1); LDB(bfn, 1, ((bslot) + 1) & 3); }   \
    PIECE(1); SB();                                                                   \
    MMA4(2, afc, bfc);                                                                \
    if (RD) { LDB(bfn, 2, ((bslot) + 1) & 3); LDB(bfn, 3, ((bslot) + 1) & 3); }       \
    PIECE(2); SB();                                                                   \
    MMA4(3, afc, bfc);                                                                \
    PIECE(3); SB();                                                                   \
    asm1 = asm1 == 5 * 16384 ? 0 : asm1 + 16384;                                      \
    as1 = as1 == 5 * 16384 ? 0 : as1 + 16384;                                         \
  } while (0)
#define K2_END(j) do { wait_v_rt(4 * (((j) + depth < 21 ? (j) + depth : 21) - ((j) + 2))); BAR(); } while (0)

  {
    const int pfirst = full ? 0 : 1;
    for (int pos = pfirst; pos < pfirst + depth; ++pos) {
      K2_SRC(pos);
      const unsigned md = m0w + (kg ? (pos & 3) : pos) * 16384;
#pragma unroll
      for (int i = 0; i < 4; ++i) dma16(md + i * 4096, voff, rs_, so_ + i * sstep);
    }
    wait_v_rt(4 * (depth - 2));
    BAR();
  }
  int as1, asm1;
  if (full) {
    LDA(af0, 0, 0); LDA(af0, 1, 0); LDA(af0, 2, 0); LDA(af0, 3, 0);
    LDB(bf0, 0, 0); LDB(bf0, 1, 0); LDB(bf0, 2, 0); LDB(bf0, 3, 0);
    as1 = 16384; asm1 = 5 * 16384;
    K2_POS(0, 0, af0, bf0, af1, bf1, true);
    K2_END(0);
  } else {
    LDA(af1, 0, 16384); LDA(af1, 1, 16384); LDA(af1, 2, 16384); LDA(af1, 3, 16384);
    LDB(bf1, 0, 1); LDB(bf1, 1, 1); LDB(bf1, 2, 1); LDB(bf1, 3, 1);
    as1 = 2 * 16384; asm1 = 0;
  }
  STAMP(1, 1);
  for (int j = 1; j < 17; j += 4) {
    STAMP(1, 2 + (j >> 2)); if (j == 1) CSTAMP(1, 28);
    K2_POS(j, 1, af1, bf1, af0, bf0, true);     K2_END(j);
    K2_POS(j + 1, 2, af0, bf0, af1, bf1, true); K2_END(j + 1);
    K2_POS(j + 2, 3, af1, bf1, af0, bf0, true); K2_END(j + 2);
    K2_POS(j + 3, 0, af0, bf0, af1, bf1, true); K2_END(j + 3);
  }
  STAMP(1, 6); CSTAMP(1, 29);
  K2_POS(17, 1, af1, bf1, af0, bf0, true); K2_END(17);
  K2_POS(18, 2, af0, bf0, af1, bf1, true); K2_END(18);
  K2_POS(19, 3, af1, bf1, af0, bf0, true); K2_END(19);
  K2_POS(20, 0, af0, bf0, af1, bf1, true);
  K2_POS(21, 1, af1, bf1, af0, bf0, false);

  STAMP(1, 7); CSTAMP(1, 31);
  {
    char* xb = smem + (w4 & 1) * 16384 + (w4 >> 1) * 65536 + lane * 16;
#pragma unroll
    for (int jj = 0; jj < 2; ++jj)
#pragma unroll
      for (int i = 0; i < 4; ++i) {
        const f32x4 v = kg ? acc[jj][i] : acc[2 + jj][i];
        *(f32x4*)(xb + kg * 8192 + (jj * 4 + i) * 1024) = v;
      }
    __syncthreads();
#pragma unroll
    for (int jj = 0; jj < 2; ++jj)
#pragma unroll
      for (int i = 0; i < 4; ++i) {
        const f32x4 o = *(const f32x4*)(xb + (kg ^ 1) * 8192 + (jj * 4 + i) * 1024);
        const f32x4 mine = kg ? acc[2 + jj][i] : acc[jj][i];
        acc[jj][i] = mine + o;
      }
  }
  const rsrc_t rs = mk_rsrc(slab, 64 * 16 * 4096 * 4);
  const unsigned lane_off = (unsigned)(wid * 2 * 64 + lane) * 16;
#pragma unroll
  for (int i = 1; i < 4; ++i) {
    const unsigned dstq = (i + p) & 3;
    const unsigned off = ((tile * 4 + dstq) * 4 + p) * 16384u + lane_off;
#pragma unroll
    for (int jj = 0; jj < 2; ++jj)
      __builtin_amdgcn_raw_buffer_store_b128(__builtin_bit_cast(u32x4, acc[jj][i]), rs, off + jj * 1024, 0, 16);
  }
  const int trw = trow * 128 + wt * 64 + p * 16 + fr;
  const int ecol = tcol * 128 + we * 64 + kg * 32 + fq * 4;
  u32x2 xr[2];
#pragma unroll
  for (int jj = 0; jj < 2; ++jj) xr[jj] = *(const u32x2*)(x + (size_t)trw * D_ + ecol + jj * 16);
  asm volatile("s_waitcnt vmcnt(0)" ::: "memory");
  __syncthreads();
  STAMP(1, 8);
  GAS unsigned* cw = (GAS unsigned*)(cnt + tile * 32);
  if (wid == 0) {
    if (lane == 0) __hip_atomic_fetch_add(cw, 1u, __ATOMIC_RELAXED, __HIP_MEMORY_SCOPE_AGENT);
    for (unsigned spins = 0; spins < (1u << 20); ++spins) {
      unsigned v = __hip_atomic_load(cw, __ATOMIC_RELAXED, __HIP_MEMORY_SCOPE_AGENT);
      if ((unsigned)__builtin_amdgcn_readfirstlane(v) >= 4u) break;
      __builtin_amdgcn_s_sleep(1);
    }
  }
  __syncthreads();
  __builtin_amdgcn_fence(__ATOMIC_ACQUIRE, "wavefront");
  STAMP(1, 9);

  u32x4 pv[3][2];
#pragma unroll
  for (int s = 1; s < 4; ++s) {
    const unsigned srcq = (s + p) & 3;
    const unsigned off = ((tile * 4 + p) * 4 + srcq) * 16384u + lane_off;
#pragma unroll
    for (int jj = 0; jj < 2; ++jj) pv[s - 1][jj] = __builtin_amdgcn_raw_buffer_load_b128(rs, off + jj * 1024, 0, 16);
  }
#pragma unroll
  for (int jj = 0; jj < 2; ++jj) {
    f32x4 v = acc[jj][0];
#pragma unroll
    for (int s = 0; s < 3; ++s) v += __builtin_bit_cast(f32x4, pv[s][jj]);
    u32x2 o;
    o[0] = pack_bf16(v[0] + bf_lo(xr[jj][0]), v[1] + bf_hi(xr[jj][0]));
    o[1] = pack_bf16(v[2] + bf_lo(xr[jj][1]), v[3] + bf_hi(xr[jj][1]));
    *(u32x2*)(out + (size_t)trw * D_ + ecol + jj * 16) = o;
  }
}

extern "C" void kernel_launch(void* const* d_in, const int* in_sizes, int n_in, void* d_out, int out_size,
                              void* d_ws, size_t ws_size, hipStream_t stream) {
  const u16* x = (const u16*)d_in[0];
  const u16* cr = (const u16*)d_in[1];
  const u16* ci = (const u16*)d_in[2];
  const u16* wr = (const u16*)d_in[3];
  const u16* wi = (const u16*)d_in[4];
  const u16* wg = (const u16*)d_in[6];
  const u16* wu = (const u16*)d_in[7];
  const u16* wd = (const u16*)d_in[8];
  char* ws = (char*)d_ws;
  u16* ab = (u16*)(ws + WS_AB);
  u16* h = (u16*)(ws + WS_H);
  float* slab = (float*)(ws + WS_SLAB);
  unsigned* cnt = (unsigned*)(ws + WS_CNT);
  (void)hipFuncSetAttribute((const void*)k1_ln_gemm, hipFuncAttributeMaxDynamicSharedMemorySize, K1_LDS);
  (void)hipFuncSetAttribute((const void*)k2_gemm, hipFuncAttributeMaxDynamicSharedMemorySize, K2_LDS);
  k1_ln_gemm<<<256, 512, K1_LDS, stream>>>(x, wr, wi, wg, wu, ab, h, cnt, cr, ci, wd);
  k2_gemm<<<256, 512, K2_LDS, stream>>>(cr, ci, h, ab, wd, x, slab, cnt, (u16*)d_out);
}
```

```cpp
#include <hip/hip_runtime.h>
#include <hip/hip_bf16.h>
#include <stdint.h>

typedef __attribute__((ext_vector_type(8))) short bf16x8;
typedef __attribute__((ext_vector_type(4))) short s16x4;
typedef __attribute__((ext_vector_type(4))) float f32x4;
typedef __attribute__((ext_vector_type(2))) float f32x2;
typedef __attribute__((ext_vector_type(4))) unsigned u32x4;
typedef __attribute__((ext_vector_type(2))) unsigned u32x2;
typedef __attribute__((ext_vector_type(2))) __bf16 bf2_t;
typedef unsigned short u16;
typedef __amdgpu_buffer_rsrc_t rsrc_t;

#define LAS __attribute__((address_space(3)))
#define GAS __attribute__((address_space(1)))

constexpr int S_ = 2048, D_ = 512, F_ = 1376;
constexpr int HLD = 2048;

constexpr size_t WS_AB = 0;
constexpr size_t WS_H = 4u << 20;
constexpr size_t WS_SLAB = 16u << 20;
constexpr size_t WS_CNT = 48u << 20;

#define WAIT_V(n) asm volatile("s_waitcnt vmcnt(" #n ")" ::: "memory")
#define WAIT_L0() asm volatile("s_waitcnt lgkmcnt(0)" ::: "memory")
#define BAR() __builtin_amdgcn_s_barrier()
#define STAMP(k, i) do {} while (0)
#define CSTAMP(k, i) do {} while (0)

__device__ __forceinline__ unsigned pack_bf16(float a, float b) {
  f32x2 v = {a, b};
  bf2_t r = __builtin_convertvector(v, bf2_t);
  return __builtin_bit_cast(unsigned, r);
}
__device__ __forceinline__ float bf_lo(unsigned u) { return __builtin_bit_cast(float, u << 16); }
__device__ __forceinline__ float bf_hi(unsigned u) { return __builtin_bit_cast(float, u & 0xffff0000u); }

__device__ __forceinline__ rsrc_t mk_rsrc(const void* p, unsigned bytes) {
  return __builtin_amdgcn_make_buffer_rsrc((void*)p, (short)0, (int)bytes, 0x00020000);
}
__device__ __forceinline__ void dma16(unsigned m0v, unsigned voff, rsrc_t rs, unsigned soff) {
  asm volatile("s_mov_b32 m0, %0\n\ts_nop 0\n\tbuffer_load_dwordx4 %1, %2, %3 offen lds"
               :: "s"(m0v), "v"(voff), "s"(rs), "s"(soff) : "memory");
}
__device__ __forceinline__ s16x4 tr_read(const char* p) {
  typedef __attribute__((ext_vector_type(4))) short v4i16_t;
  return __builtin_amdgcn_ds_read_tr16_b64_v4i16((LAS v4i16_t*)p);
}
__device__ __forceinline__ bf16x8 cat8(s16x4 a, s16x4 b) {
  return __builtin_shufflevector(a, b, 0, 1, 2, 3, 4, 5, 6, 7);
}
__device__ __forceinline__ float gelu_tanh(float x) {
  float u = x * (0.7978845608028654f + 0.035677408136300125f * x * x);
  float e = __builtin_amdgcn_exp2f(-2.885390081777927f * u);
  return x * __builtin_amdgcn_rcpf(1.0f + e);
}

#define SB() __builtin_amdgcn_sched_barrier(0)
template <int N> __device__ __forceinline__ void wait_v() { asm volatile("s_waitcnt vmcnt(%0)" :: "i"(N) : "memory"); }
template <int N> __device__ __forceinline__ void wait_l() { asm volatile("s_waitcnt lgkmcnt(%0)" :: "i"(N) : "memory"); }

constexpr int K1_NS = 6, K1_B_OFF = K1_NS * 8192, K1_X_OFF = K1_NS * 24576;
#define K1_PF_POS 13
constexpr int K1_LDS = K1_X_OFF + 8192;

__device__ __forceinline__ void pf_load(unsigned m0v, unsigned voff, rsrc_t rs, unsigned soff) {
  asm volatile("s_mov_b32 m0, %0\n\ts_nop 0\n\tbuffer_load_dword %1, %2, %3 offen lds"
               :: "s"(m0v), "v"(voff), "s"(rs), "s"(soff) : "memory");
}
__device__ __forceinline__ void k1_pf_far(unsigned m0v, int bid, int w, const u16* cr, const u16* ci) {
  pf_load(m0v, (threadIdx.x & 63) * 128, mk_rsrc(bid < 128 ? cr : ci, S_ * S_ * 2), ((bid & 127) * 8 + w) * 8192u);
}

template <bool TR>
__device__ __forceinline__ void k1_body(char* smem, const u16* __restrict__ x, const u16* __restrict__ w0,
                                        const u16* __restrict__ w1, u16* __restrict__ outp, int rb, int ncol0,
                                        float sgn, const u16* cr, const u16* ci, const u16* wd) {
  const int tid = threadIdx.x;
  const int lane = tid & 63, wid = __builtin_amdgcn_readfirstlane(tid >> 6), wm = wid >> 2, wn = wid & 3;
  int fr = lane & 15, fq = lane >> 4;
  const unsigned ldsb = (unsigned)(size_t)(LAS char*)smem;

  STAMP(0, 0); CSTAMP(0, 30);
  const unsigned rvoff = (lane >> 2) * (D_ * 2) + (((lane & 3) ^ ((-(lane >> 4)) & 3)) * 16);
  const rsrc_t rsx = mk_rsrc(x, S_ * D_ * 2);
  const unsigned asoff = (rb * 128 + 16 * wid) * (D_ * 2);
  unsigned bvoff, bsoff;
  rsrc_t rs0, rs1;
  if constexpr (!TR) {
    bvoff = rvoff;
    bsoff = (ncol0 + 16 * wid) * (D_ * 2);
    rs0 = mk_rsrc(w0, D_ * D_ * 2);
    rs1 = rs0;
  } else {
    const int f = (lane >> 4) | (((wid >> 1) & 1) << 2);
    bvoff = (lane >> 4) * (F_ * 2) + (((lane & 15) ^ (f << 1)) * 16);
    bsoff = 4 * wid * (F_ * 2) + ncol0 * 2;
    rs0 = mk_rsrc(w0, D_ * F_ * 2);
    rs1 = mk_rsrc(w1, D_ * F_ * 2);
  }
  const unsigned m0w = ldsb + wid * 1024;
#define K1_ROT(rb, ncol0) (((rb) + ((ncol0) >> 7) * 3) & 15)
  const int rot = K1_ROT(rb, ncol0);
#define K1_PIECE(t, i)                                                                                    \
  do {                                                                                                    \
    const unsigned m0_ = m0w + ((i) == 0 ? ((t) % K1_NS) * 8192 : K1_B_OFF + ((t) % K1_NS) * 16384 + ((i) - 1) * 8192); \
    const int tr_ = ((t) + rot) & 15;                                                                     \
    if ((i) == 0) dma16(m0_, rvoff, rsx, asoff + tr_ * 64);                                               \
    else if constexpr (!TR) dma16(m0_, bvoff, rs0, bsoff + ((i) - 1) * (128 * D_ * 2) + tr_ * 64);        \
    else dma16(m0_, bvoff, (i) == 2 ? rs1 : rs0, bsoff + tr_ * (32 * F_ * 2));                            \
  } while (0)
#pragma unroll
  for (int t = 0; t < K1_NS - 1; ++t) { K1_PIECE(t, 0); K1_PIECE(t, 1); K1_PIECE(t, 2); }

  const int rsw = (fq ^ ((-(fr >> 2)) & 3)) << 4;
  int a_rd[4], b_rd[4];
#pragma unroll
  for (int i = 0; i < 4; ++i) a_rd[i] = (wm * 64 + ((i + wn) & 3) * 16 + fr) * 64 + rsw;
#pragma unroll
  for (int j = 0; j < 4; ++j) {
    const int ja = (j + 2 * wm) & 3;
    if constexpr (!TR) {
      b_rd[j] = K1_B_OFF + (wn * 64 + ja * 16 + fr) * 64 + rsw;
    } else {
      const int q = fr >> 2, pp = fr & 3, f = q | ((fq & 1) << 2);
      const int ch = 4 * wn + 2 * (ja & 1) + (pp >> 1);
      b_rd[j] = K1_B_OFF + (ja >> 1) * 8192 + (8 * fq + q) * 256 + ((ch ^ (f << 1)) << 4) + (pp & 1) * 8;
    }
  }

  f32x4 acc[4][4], acs[2], ars, agr;
#pragma unroll
  for (int j = 0; j < 4; ++j)
#pragma unroll
    for (int i = 0; i < 4; ++i) acc[j][i] = (f32x4){0.f, 0.f, 0.f, 0.f};
  acs[0] = acs[1] = ars = agr = (f32x4){0.f, 0.f, 0.f, 0.f};
  const bf16x8 ones = {0x3F80, 0x3F80, 0x3F80, 0x3F80, 0x3F80, 0x3F80, 0x3F80, 0x3F80};

  bf16x8 af[2][4], bfr[2][4];
#define K1_LDA(t, i) af[(t) & 1][i] = *(const bf16x8*)(smem + ((t) % K1_NS) * 8192 + a_rd[i])
#define K1_LDB(t, j)                                                                                      \
  do {                                                                                                    \
    const char* p_ = smem + ((t) % K1_NS) * 16384 + b_rd[j];                                              \
    if constexpr (!TR) bfr[(t) & 1][j] = *(const bf16x8*)p_;                                              \
    else bfr[(t) & 1][j] = cat8(tr_read(p_), tr_read(p_ + 4 * 256));                                      \
  } while (0)
#define MFMA(a, b, c) __builtin_amdgcn_mfma_f32_16x16x32_bf16(a, b, c, 0, 0, 0)
#define K1_MMA4(t, j)                                                                                     \
  _Pragma("unroll") for (int i_ = 0; i_ < 4; ++i_) acc[j][i_] = MFMA(bfr[(t) & 1][j], af[(t) & 1][i_], acc[j][i_])

  wait_v<3 * (K1_NS - 3)>();
  BAR();
  STAMP(0, 2); CSTAMP(0, 28);
#pragma unroll
  for (int i = 0; i < 4; ++i) K1_LDA(0, i);
#pragma unroll
  for (int j = 0; j < 4; ++j) K1_LDB(0, j);

#pragma unroll
  for (int t = 0; t < 16; ++t) {
    K1_MMA4(t, 0);
    acs[0] = MFMA(bfr[t & 1][0], ones, acs[0]);
    if (t + 1 < 16) { K1_LDA(t + 1, 0); K1_LDA(t + 1, 1); K1_LDB(t + 1, 0); }
    if (t + K1_NS - 1 < 16) K1_PIECE(t + K1_NS - 1, 0);
    SB();
    K1_MMA4(t, 1);
    acs[1] = MFMA(bfr[t & 1][1], ones, acs[1]);
    if (t + 1 < 16) { K1_LDA(t + 1, 2); K1_LDA(t + 1, 3); K1_LDB(t + 1, 1); }
    if (t + K1_NS - 1 < 16) K1_PIECE(t + K1_NS - 1, 1);
    SB();
    K1_MMA4(t, 2);
    ars = MFMA(ones, af[t & 1][0], ars);
    if (t + 1 < 16) { K1_LDB(t + 1, 2); K1_LDB(t + 1, 3); }
    if (t + K1_NS - 1 < 16) K1_PIECE(t + K1_NS - 1, 2);
    SB();
    K1_MMA4(t, 3);
    agr = MFMA(af[t & 1][0], af[t & 1][0], agr);
    if (t == K1_PF_POS) k1_pf_far(ldsb + K1_X_OFF + 4096 + wid * 256, blockIdx.x, wid, cr, ci);
    SB();
    if (t + 2 < 16) {
      const int last = t + K1_NS - 1 < 15 ? t + K1_NS - 1 : 15;
      const int n = 3 * (last - (t + 2)) + ((t >= K1_PF_POS && t + 2 <= K1_PF_POS + K1_NS - 1) ? 1 : 0);
      switch (n) {
        case 10: wait_v<10>(); break;
        case 9: wait_v<9>(); break;
        case 7: wait_v<7>(); break;
        case 6: wait_v<6>(); break;
        case 4: wait_v<4>(); break;
        case 3: wait_v<3>(); break;
        case 1: wait_v<1>(); break;
        default: wait_v<0>(); break;
      }
      BAR();
    }
    if (t == 3 || t == 7 || t == 11) STAMP(0, 3 + (t >> 2));
    if (t == 11) CSTAMP(0, 29);
  }

  STAMP(0, 10); CSTAMP(0, 31);
  {
    int tz = threadIdx.x;
    asm volatile("" : "+v"(tz));
    fr = tz & 15; fq = (tz >> 4) & 3;
  }
  float* xs = (float*)(smem + K1_X_OFF);
  {
    const float d = (fr & 2) ? ((fr & 1) ? agr[3] : agr[2]) : ((fr & 1) ? agr[1] : agr[0]);
    const float mean = ars[0] * (1.0f / D_);
    const float var = fmaxf(d * (1.0f / D_) - mean * mean, 0.f);
    if (fq == (fr >> 2)) {
      xs[wm * 64 + wn * 16 + fr] = mean;
      xs[128 + wm * 64 + wn * 16 + fr] = __builtin_amdgcn_rsqf(var + 1e-5f);
    }
    if (fr == 0) {
      *(f32x4*)(xs + 256 + wn * 64 + (2 * wm) * 16 + fq * 4) = acs[0];
      *(f32x4*)(xs + 256 + wn * 64 + (2 * wm + 1) * 16 + fq * 4) = acs[1];
    }
  }
  __syncthreads();
  f32x4 cs[4];
#pragma unroll
  for (int j = 0; j < 4; ++j) cs[j] = *(const f32x4*)(xs + 256 + wn * 64 + ((j + 2 * wm) & 3) * 16 + fq * 4);

#pragma unroll
  for (int i = 0; i < 4; ++i) {
    const int mrow = wm * 64 + ((i + wn) & 3) * 16 + fr;
    const float mean = xs[mrow], rstd = xs[128 + mrow];
    if constexpr (!TR) {
      u16* op = outp + (size_t)(rb * 128 + mrow) * D_ + ncol0 + wn * 64 + fq * 4;
      const float rs = rstd * sgn;
#pragma unroll
      for (int j = 0; j < 4; ++j) {
        const f32x4 v = (acc[j][i] - mean * cs[j]) * rs;
        u32x2 o;
        o[0] = pack_bf16(v[0], v[1]);
        o[1] = pack_bf16(v[2], v[3]);
        *(u32x2*)(op + ((j + 2 * wm) & 3) * 16) = o;
      }
    } else {
      const int fcol = ncol0 + wn * 32;
      const bool valid = fcol < F_;
      u16* op = outp + (size_t)(rb * 128 + mrow) * HLD + fcol + fq * 4;
#pragma unroll
      for (int jj = 0; jj < 2; ++jj) {
        u32x2 o = {0u, 0u};
        if (valid) {
          const f32x4 v0 = (acc[jj][i] - mean * cs[jj]) * rstd;
          const f32x4 v1 = (acc[2 + jj][i] - mean * cs[2 + jj]) * rstd;
          float v[4];
#pragma unroll
          for (int r = 0; r < 4; ++r) v[r] = gelu_tanh(wm ? v1[r] : v0[r]) * (wm ? v0[r] : v1[r]);
          o[0] = pack_bf16(v[0], v[1]);
          o[1] = pack_bf16(v[2], v[3]);
        }
        *(u32x2*)(op + jj * 16) = o;
      }
    }
  }
}

__global__ __launch_bounds__(512, 2) void k1_ln_gemm(const u16* __restrict__ x, const u16* __restrict__ wr,
                                                     const u16* __restrict__ wi, const u16* __restrict__ wg,
                                                     const u16* __restrict__ wu, u16* __restrict__ ab,
                                                     u16* __restrict__ h, unsigned* __restrict__ cnt,
                                                     const u16* __restrict__ cr, const u16* __restrict__ ci,
                                                     const u16* __restrict__ wd) {
  extern __shared__ __attribute__((aligned(16))) char smem[];
  const int bid = blockIdx.x;
  if (bid >= 240) {
    if (bid == 240 && threadIdx.x < 64) cnt[threadIdx.x * 32] = 0u;
    const int w = __builtin_amdgcn_readfirstlane(threadIdx.x >> 6);
    const unsigned m0v = (unsigned)(size_t)(LAS char*)smem + w * 256;
    const rsrc_t rw = mk_rsrc(wd, F_ * D_ * 2);
    pf_load(m0v, (threadIdx.x & 63) * 128, rw, ((bid - 240) * 16 + w) * 8192u);
    pf_load(m0v, (threadIdx.x & 63) * 128, rw, ((bid - 240) * 16 + 8 + w) * 8192u);
    k1_pf_far(m0v, bid, w, cr, ci);
    wait_v<0>();
    return;
  }
  const int id = (bid & 7) * 30 + (bid >> 3);
  const int ct = id >> 4, rb = id & 15;
  if (ct < 4) {
    k1_body<false>(smem, x, ct < 2 ? wr : wi, nullptr, ab + (ct < 2 ? 0 : (size_t)S_ * D_), rb, (ct & 1) * 256,
                   ct < 2 ? 1.0f : -1.0f, cr, ci, wd);
  } else {
    k1_body<true>(smem, x, wg, wu, h, rb, (ct - 4) * 128, 1.0f, cr, ci, wd);
  }
}

#define K2_NS 5
constexpr int K2_B_OFF = K2_NS * 16384, K2_LDS = 2 * K2_NS * 16384;

__global__ __launch_bounds__(768) void k2_gemm(const u16* __restrict__ cr, const u16* __restrict__ ci,
                                                  const u16* __restrict__ h, const u16* __restrict__ ab,
                                                  const u16* __restrict__ wd, const u16* __restrict__ x,
                                                  float* __restrict__ slab, unsigned* __restrict__ cnt,
                                                  u16* __restrict__ out) {
  extern __shared__ __attribute__((aligned(16))) char smem[];
  const int tid = threadIdx.x, lane = tid & 63, wid = __builtin_amdgcn_readfirstlane(tid >> 6);
  const int kg = wid >> 2, wt = (wid >> 1) & 1, we = wid & 1, w4 = wid & 3;
  const int fr = lane & 15, fq = lane >> 4;
  const unsigned ldsb = (unsigned)(size_t)(LAS char*)smem;
  const int bid = blockIdx.x;
  const int xcd = bid & 7, loc = bid >> 3;
  const int p = xcd >> 1;
  const int trow = (xcd & 1) * 8 + (loc >> 2), tcol = loc & 3;
  const int tile = trow * 4 + tcol;
  const bool full = p < 2;
  const int cb = p < 2 ? p * 22 : 44 + (p - 2) * 21;
  const int nch = full ? 22 : 21;
#define K2_ROT(trow) (((trow) * 5) % 21)
  const int rot = K2_ROT(trow) - (full ? 0 : 1);

  STAMP(1, 0); CSTAMP(1, 30);
  if (wid >= 8) {
    const unsigned avoff = (lane >> 3) * 4096u + (((lane & 7) ^ ((((w4 & 1) << 2) + (lane >> 4)) & 7)) * 16);
    const unsigned bvoff = (lane >> 4) * 1024u + (((lane & 15) ^ (((lane >> 4) | (((w4 >> 1) & 1) << 2)) << 1)) * 16);
    const unsigned asoff0 = (unsigned)(trow * 128 + w4 * 8) * 4096u;
    const unsigned bsoff0 = (unsigned)tcol * 256u + w4 * 4096u;
    const unsigned m0w = ldsb + w4 * 1024;
    const int pfirst = full ? 0 : 1;
    unsigned slot = pfirst * 16384;
    for (int pos = pfirst; pos < 22 + K2_NS - 3; ++pos) {
      if (pos < 22) {
        const int cr_ = pos + rot;
        const int c_ = cb + (cr_ >= nch ? cr_ - nch : cr_);
        const rsrc_t ra_ = mk_rsrc(c_ < 32 ? cr : (c_ < 64 ? ci : h), S_ * S_ * 2);
        const rsrc_t rb_ = mk_rsrc(c_ < 64 ? ab : wd, c_ < 64 ? 2 * S_ * D_ * 2 : F_ * D_ * 2);
        const unsigned sa_ = asoff0 + (unsigned)(c_ & 31) * 128u, sb_ = bsoff0 + (unsigned)(c_ & 63) * 65536u;
#pragma unroll
        for (int i = 0; i < 4; ++i) {
          dma16(m0w + slot + i * 4096, avoff, ra_, sa_ + i * (32u * 4096u));
          dma16(m0w + K2_B_OFF + slot + i * 4096, bvoff, rb_, sb_ + i * (16u * 1024u));
        }
        slot = slot == (K2_NS - 1) * 16384 ? 0 : slot + 16384;
      }
      if (pos >= pfirst + K2_NS - 2) {
        if (pos < 22) wait_v<8 * (K2_NS - 3)>();
        else if (pos == 22 && K2_NS == 5) wait_v<8>();
        else wait_v<0>();
        BAR();
      }
    }
    return;
  }

  const int asw = (fr >> 1) & 7;
  int a_rd[4];
#pragma unroll
  for (int i = 0; i < 4; ++i) a_rd[i] = (wt * 64 + ((i + p) & 3) * 16 + fr) * 128 + (((fq + 4 * kg) ^ asw) << 4);
  int b_rd[4];
  {
    const int q = fr >> 2, pp = fr & 3, f = q | ((fq & 1) << 2);
#pragma unroll
    for (int j = 0; j < 4; ++j) {
      const int ch = 8 * we + 2 * j + (pp >> 1);
      b_rd[j] = K2_B_OFF + (32 * kg + 8 * fq + q) * 256 + ((ch ^ (f << 1)) << 4) + (pp & 1) * 8;
    }
  }

  f32x4 acc[4][4];
#pragma unroll
  for (int j = 0; j < 4; ++j)
#pragma unroll
    for (int i = 0; i < 4; ++i) acc[j][i] = (f32x4){0.f, 0.f, 0.f, 0.f};

  bf16x8 af0[4], bf0[4], af1[4], bf1[4];
#define LDA(dst, i, so) dst[i] = *(const bf16x8*)(smem + (so) + a_rd[i])
#define LDB(dst, j, so)                                                               \
  do {                                                                                \
    const char* pb_ = smem + (so) + b_rd[j];                                          \
    dst[j] = cat8(tr_read(pb_), tr_read(pb_ + 4 * 256));                              \
  } while (0)
#define MMA4(j, af, bfr)                                                              \
  _Pragma("unroll") for (int i_ = 0; i_ < 4; ++i_) acc[j][i_] = MFMA(bfr[j], af[i_], acc[j][i_])
#define K2_POS(afc, bfc, afn, bfn, RD)                                                \
  do {                                                                                \
    MMA4(0, afc, bfc);                                                                \
    if (RD) { LDA(afn, 0, s1); LDA(afn, 1, s1); LDB(bfn, 0, s1); }                    \
    SB();                                                                             \
    MMA4(1, afc, bfc);                                                                \
    if (RD) { LDA(afn, 2, s1); LDA(afn, 3, s1); LDB(bfn, 1, s1); }                    \
    SB();                                                                             \
    MMA4(2, afc, bfc);                                                                \
    if (RD) { LDB(bfn, 2, s1); LDB(bfn, 3, s1); }                                     \
    SB();                                                                             \
    MMA4(3, afc, bfc);                                                                \
    SB();                                                                             \
    s1 = s1 == (K2_NS - 1) * 16384 ? 0 : s1 + 16384;                                  \
  } while (0)

  BAR();
  int s1;
  if (full) {
    LDA(af0, 0, 0); LDA(af0, 1, 0); LDA(af0, 2, 0); LDA(af0, 3, 0);
    LDB(bf0, 0, 0); LDB(bf0, 1, 0); LDB(bf0, 2, 0); LDB(bf0, 3, 0);
    s1 = 16384;
    K2_POS(af0, bf0, af1, bf1, true);
    BAR();
  } else {
    LDA(af1, 0, 16384); LDA(af1, 1, 16384); LDA(af1, 2, 16384); LDA(af1, 3, 16384);
    LDB(bf1, 0, 16384); LDB(bf1, 1, 16384); LDB(bf1, 2, 16384); LDB(bf1, 3, 16384);
    s1 = 2 * 16384;
  }
  STAMP(1, 1); STAMP(1, 2); CSTAMP(1, 28);
  for (int j = 1; j < 19; j += 2) {
    K2_POS(af1, bf1, af0, bf0, true); BAR();
    K2_POS(af0, bf0, af1, bf1, true); BAR();
  }
  K2_POS(af1, bf1, af0, bf0, true); BAR();
  K2_POS(af0, bf0, af1, bf1, true);
  K2_POS(af1, bf1, af0, bf0, false);

  STAMP(1, 7); CSTAMP(1, 31);
  {
    char* xb = smem + (2 + (w4 & 1)) * 16384 + (w4 >> 1) * K2_B_OFF + lane * 16;
#pragma unroll
    for (int jj = 0; jj < 2; ++jj)
#pragma unroll
      for (int i = 0; i < 4; ++i) {
        const f32x4 v = kg ? acc[jj][i] : acc[2 + jj][i];
        *(f32x4*)(xb + kg * 8192 + (jj * 4 + i) * 1024) = v;
      }
    __syncthreads();
#pragma unroll
    for (int jj = 0; jj < 2; ++jj)
#pragma unroll
      for (int i = 0; i < 4; ++i) {
        const f32x4 o = *(const f32x4*)(xb + (kg ^ 1) * 8192 + (jj * 4 + i) * 1024);
        const f32x4 mine = kg ? acc[2 + jj][i] : acc[jj][i];
        acc[jj][i] = mine + o;
      }
  }
  const rsrc_t rs = mk_rsrc(slab, 64 * 16 * 8192);
  const unsigned lane_off = (unsigned)(wid * 64 + lane) * 16;
#pragma unroll
  for (int i = 1; i < 4; ++i) {
    const unsigned dstq = (i + p) & 3;
    const unsigned off = ((tile * 4 + dstq) * 4 + p) * 8192u + lane_off;
    u32x4 v;
    v[0] = pack_bf16(acc[0][i][0], acc[0][i][1]);
    v[1] = pack_bf16(acc[0][i][2], acc[0][i][3]);
    v[2] = pack_bf16(acc[1][i][0], acc[1][i][1]);
    v[3] = pack_bf16(acc[1][i][2], acc[1][i][3]);
    __builtin_amdgcn_raw_buffer_store_b128(v, rs, off, 0, 16);
  }
  const int trw = trow * 128 + wt * 64 + p * 16 + fr;
  const int ecol = tcol * 128 + we * 64 + kg * 32 + fq * 4;
  u32x2 xr[2];
#pragma unroll
  for (int jj = 0; jj < 2; ++jj) xr[jj] = *(const u32x2*)(x + (size_t)trw * D_ + ecol + jj * 16);
  asm volatile("s_waitcnt vmcnt(0)" ::: "memory");
  __syncthreads();
  STAMP(1, 8);
  GAS unsigned* cw = (GAS unsigned*)(cnt + tile * 32);
  if (wid == 0) {
    if (lane == 0) __hip_atomic_fetch_add(cw, 1u, __ATOMIC_RELAXED, __HIP_MEMORY_SCOPE_AGENT);
    for (unsigned spins = 0; spins < (1u << 20); ++spins) {
      unsigned v = __hip_atomic_load(cw, __ATOMIC_RELAXED, __HIP_MEMORY_SCOPE_AGENT);
      if ((unsigned)__builtin_amdgcn_readfirstlane(v) >= 4u) break;
      __builtin_amdgcn_s_sleep(1);
    }
  }
  __syncthreads();
  __builtin_amdgcn_fence(__ATOMIC_ACQUIRE, "wavefront");
  STAMP(1, 9);

  u32x4 pv[3];
#pragma unroll
  for (int s = 1; s < 4; ++s) {
    const unsigned srcq = (s + p) & 3;
    pv[s - 1] = __builtin_amdgcn_raw_buffer_load_b128(rs, ((tile * 4 + p) * 4 + srcq) * 8192u + lane_off, 0, 16);
  }
#pragma unroll
  for (int jj = 0; jj < 2; ++jj) {
    f32x4 v = acc[jj][0];
#pragma unroll
    for (int s = 0; s < 3; ++s) {
      v[0] += bf_lo(pv[s][2 * jj]);
      v[1] += bf_hi(pv[s][2 * jj]);
      v[2] += bf_lo(pv[s][2 * jj + 1]);
      v[3] += bf_hi(pv[s][2 * jj + 1]);
    }
    u32x2 o;
    o[0] = pack_bf16(v[0] + bf_lo(xr[jj][0]), v[1] + bf_hi(xr[jj][0]));
    o[1] = pack_bf16(v[2] + bf_lo(xr[jj][1]), v[3] + bf_hi(xr[jj][1]));
    *(u32x2*)(out + (size_t)trw * D_ + ecol + jj * 16) = o;
  }
}

extern "C" void kernel_launch(void* const* d_in, const int* in_sizes, int n_in, void* d_out, int out_size,
                              void* d_ws, size_t ws_size, hipStream_t stream) {
  const u16* x = (const u16*)d_in[0];
  const u16* cr = (const u16*)d_in[1];
  const u16* ci = (const u16*)d_in[2];
  const u16* wr = (const u16*)d_in[3];
  const u16* wi = (const u16*)d_in[4];
  const u16* wg = (const u16*)d_in[6];
  const u16* wu = (const u16*)d_in[7];
  const u16* wd = (const u16*)d_in[8];
  char* ws = (char*)d_ws;
  u16* ab = (u16*)(ws + WS_AB);
  u16* h = (u16*)(ws + WS_H);
  float* slab = (float*)(ws + WS_SLAB);
  unsigned* cnt = (unsigned*)(ws + WS_CNT);
  (void)hipFuncSetAttribute((const void*)k1_ln_gemm, hipFuncAttributeMaxDynamicSharedMemorySize, K1_LDS);
  (void)hipFuncSetAttribute((const void*)k2_gemm, hipFuncAttributeMaxDynamicSharedMemorySize, K2_LDS);
  k1_ln_gemm<<<256, 512, K1_LDS, stream>>>(x, wr, wi, wg, wu, ab, h, cnt, cr, ci, wd);
  k2_gemm<<<256, 768, K2_LDS, stream>>>(cr, ci, h, ab, wd, x, slab, cnt, (u16*)d_out);
}
```

```cpp
#include <hip/hip_runtime.h>
#include <hip/hip_bf16.h>
#include <stdint.h>

typedef __attribute__((ext_vector_type(8))) short bf16x8;
typedef __attribute__((ext_vector_type(4))) short s16x4;
typedef __attribute__((ext_vector_type(4))) float f32x4;
typedef __attribute__((ext_vector_type(2))) float f32x2;
typedef __attribute__((ext_vector_type(4))) unsigned u32x4;
typedef __attribute__((ext_vector_type(2))) unsigned u32x2;
typedef __attribute__((ext_vector_type(2))) __bf16 bf2_t;
typedef unsigned short u16;
typedef unsigned long long u64;
typedef __amdgpu_buffer_rsrc_t rsrc_t;

#define LAS __attribute__((address_space(3)))
#define GAS __attribute__((address_space(1)))

constexpr int S_ = 2048, D_ = 512, F_ = 1376;
constexpr int HLD = 2048;

constexpr size_t WS_AB = 0;
constexpr size_t WS_H = 4u << 20;
constexpr size_t WS_SLAB = 16u << 20;
constexpr size_t WS_FLAG = 48u << 20;
constexpr size_t WS_STAT = 49u << 20;
constexpr u64 MAGIC = 0x9E3779B97F4A7C15ull;

#define BAR() __builtin_amdgcn_s_barrier()
#define SB() __builtin_amdgcn_sched_barrier(0)
template <int N> __device__ __forceinline__ void wait_v() { asm volatile("s_waitcnt vmcnt(%0)" :: "i"(N) : "memory"); }
__device__ __forceinline__ void wait_l0() { asm volatile("s_waitcnt lgkmcnt(0)" ::: "memory"); }

__device__ __forceinline__ unsigned pack_bf16(float a, float b) {
  f32x2 v = {a, b};
  bf2_t r = __builtin_convertvector(v, bf2_t);
  return __builtin_bit_cast(unsigned, r);
}
__device__ __forceinline__ float bf_lo(unsigned u) { return __builtin_bit_cast(float, u << 16); }
__device__ __forceinline__ float bf_hi(unsigned u) { return __builtin_bit_cast(float, u & 0xffff0000u); }

__device__ __forceinline__ rsrc_t mk_rsrc(const void* p, unsigned bytes) {
  return __builtin_amdgcn_make_buffer_rsrc((void*)p, (short)0, (int)bytes, 0x00020000);
}
__device__ __forceinline__ void dma16(unsigned m0v, unsigned voff, rsrc_t rs, unsigned soff) {
  asm volatile("s_mov_b32 m0, %0\n\ts_nop 0\n\tbuffer_load_dwordx4 %1, %2, %3 offen lds"
               :: "s"(m0v), "v"(voff), "s"(rs), "s"(soff) : "memory");
}
__device__ __forceinline__ void dma16c(unsigned m0v, unsigned voff, rsrc_t rs, unsigned soff) {
  asm volatile("s_mov_b32 m0, %0\n\ts_nop 0\n\tbuffer_load_dwordx4 %1, %2, %3 offen sc1 lds"
               :: "s"(m0v), "v"(voff), "s"(rs), "s"(soff) : "memory");
}
__device__ __forceinline__ void pf_load(unsigned m0v, unsigned voff, rsrc_t rs, unsigned soff) {
  asm volatile("s_mov_b32 m0, %0\n\ts_nop 0\n\tbuffer_load_dword %1, %2, %3 offen lds"
               :: "s"(m0v), "v"(voff), "s"(rs), "s"(soff) : "memory");
}
__device__ __forceinline__ s16x4 tr_read(const char* p) {
  typedef __attribute__((ext_vector_type(4))) short v4i16_t;
  return __builtin_amdgcn_ds_read_tr16_b64_v4i16((LAS v4i16_t*)p);
}
__device__ __forceinline__ bf16x8 cat8(s16x4 a, s16x4 b) {
  return __builtin_shufflevector(a, b, 0, 1, 2, 3, 4, 5, 6, 7);
}
__device__ __forceinline__ f32x4 geglu4(f32x4 g, f32x4 u) {
  const f32x4 a = g * (g * g * -0.10294324f + -2.3022082f);
  f32x4 d;
#pragma unroll
  for (int r = 0; r < 4; ++r) d[r] = __builtin_amdgcn_exp2f(a[r]);
  d = d + 1.0f;
#pragma unroll
  for (int r = 0; r < 4; ++r) d[r] = __builtin_amdgcn_rcpf(d[r]);
  return (g * u) * d;
}
#define MFMA(a, b, c) __builtin_amdgcn_mfma_f32_16x16x32_bf16(a, b, c, 0, 0, 0)

__device__ __forceinline__ void flag_set(u64* flags, int dst, int slot) {
  __hip_atomic_store((GAS u64*)(flags + dst * 16 + slot), MAGIC, __ATOMIC_RELAXED, __HIP_MEMORY_SCOPE_AGENT);
}
__device__ __forceinline__ void flags_wait(u64* flags, int bid, int first, int n, int lane) {
  GAS u64* w = (GAS u64*)(flags + bid * 16 + first + (lane < n ? lane : 0));
  for (unsigned spins = 0; spins < (1u << 18); ++spins) {
    const u64 v = __hip_atomic_load(w, __ATOMIC_RELAXED, __HIP_MEMORY_SCOPE_AGENT);
    if (__all(v == MAGIC)) break;
    __builtin_amdgcn_s_sleep(1);
  }
  __builtin_amdgcn_fence(__ATOMIC_ACQUIRE, "wavefront");
}
__device__ __forceinline__ int cons_bid(int p, int trow, int tcol) { return ((trow & 7) * 4 + tcol) * 8 + 2 * p + (trow >> 3); }

constexpr int K1_B_OFF = 3 * 16384;
constexpr int XS_OFF = 144 * 1024;
constexpr int PF_OFF = 156 * 1024;
constexpr int STG_OFF = 64 * 1024;
constexpr int K2_NS = 4, K2_B_OFF = K2_NS * 16384;
constexpr int LDS_BYTES = 160 * 1024;

template <bool TR>
__device__ __forceinline__ void p1_loader(unsigned ldsb, int lw, int lane, const u16* x, const u16* w0, const u16* w1,
                                          int rb, int ncol0, int ct, bool fetch, const float* stat, u64* flags) {
  const unsigned rvoff = (lane >> 3) * (D_ * 2) + (((lane & 7) ^ ((((lw & 1) << 2) + (lane >> 4)) & 7)) * 16);
  const rsrc_t rsx = mk_rsrc(x, S_ * D_ * 2);
  const unsigned asoff = (rb * 128 + 8 * lw) * (D_ * 2);
  unsigned bvoff, bsoff;
  rsrc_t rs0, rs1;
  if constexpr (!TR) {
    bvoff = rvoff;
    bsoff = (ncol0 + 8 * lw) * (D_ * 2);
    rs0 = mk_rsrc(w0, D_ * D_ * 2);
    rs1 = rs0;
  } else {
    bvoff = (lane >> 4) * (F_ * 2) + (((lane & 15) ^ (((lane >> 4) | (((lw >> 1) & 1) << 2)) << 1)) * 16);
    bsoff = 4 * lw * (F_ * 2) + ncol0 * 2;
    rs0 = mk_rsrc(w0, D_ * F_ * 2);
    rs1 = mk_rsrc(w1, D_ * F_ * 2);
  }
  const unsigned m0w = ldsb + lw * 1024;
  const int rot = (rb + (ncol0 >> 7) * 3) & 7;
#define P1_B(i, sl, cc)                                                                                        \
  do {                                                                                                         \
    if constexpr (!TR) dma16(m0w + K1_B_OFF + (sl) * 32768 + (i) * 4096, bvoff, rs0, bsoff + (i) * (32 * D_ * 2) + (cc) * 128); \
    else dma16(m0w + K1_B_OFF + (sl) * 32768 + (i) * 4096, bvoff, (i) < 4 ? rs0 : rs1, bsoff + ((cc) * 64 + ((i) & 3) * 16) * (F_ * 2)); \
  } while (0)
#define P1_HALF(hf, C, sl)                                                                                     \
  do {                                                                                                         \
    const int cc_ = ((C) + rot) & 7;                                                                           \
    if ((hf) == 0) {                                                                                           \
      _Pragma("unroll") for (int i = 0; i < 4; ++i) dma16(m0w + (sl) * 16384 + i * 4096, rvoff, rsx, asoff + i * (32 * D_ * 2) + cc_ * 128); \
      P1_B(0, sl, cc_); P1_B(1, sl, cc_);                                                                      \
    } else {                                                                                                   \
      P1_B(2, sl, cc_); P1_B(3, sl, cc_); P1_B(4, sl, cc_); P1_B(5, sl, cc_); P1_B(6, sl, cc_); P1_B(7, sl, cc_); \
    }                                                                                                          \
  } while (0)
  if (!fetch) {
#pragma unroll
    for (int C = 0; C < 2; ++C) { P1_HALF(0, C, C); P1_HALF(1, C, C); }
    int sl = 2;
#pragma unroll 1
    for (int C = 0; C < 8; ++C) {
      if (C < 7) wait_v<12>(); else wait_v<0>();
      BAR();
      if (C + 2 < 8) { P1_HALF(0, C + 2, sl); P1_HALF(1, C + 2, sl); }
      sl = sl == 2 ? 0 : sl + 1;
    }
    return;
  }
#pragma unroll
  for (int C = 0; C < 2; ++C) { P1_HALF(0, C, C); P1_HALF(1, C, C); }
  wait_v<12>(); BAR();
  P1_HALF(0, 2, 2);
  wait_v<6>(); BAR();
  P1_HALF(1, 2, 2);
  BAR();
  int sl = 0;
#pragma unroll 1
  for (int C = 3; C < 8; ++C) {
    P1_HALF(0, C, sl);
    wait_v<6>(); BAR();
    P1_HALF(1, C, sl);
    BAR();
    sl = sl == 2 ? 0 : sl + 1;
  }
  wait_v<0>(); BAR();
  if (fetch && lw == 0) {
    flags_wait(flags, blockIdx.x, 14, 2, lane);
    const rsrc_t rs = mk_rsrc(stat, 31 * 1024);
    dma16c(ldsb + XS_OFF, lane * 16, rs, rb * 1024);
    dma16c(ldsb + XS_OFF + 1024, lane * 16, rs, (16 + ct) * 1024);
  }
  BAR();
  wait_v<0>();
}

__device__ __forceinline__ void warm_up(unsigned ldsb, int w, const u16* cr, const u16* ci, const u16* wd) {
  const int bid = blockIdx.x;
  const unsigned vo = (threadIdx.x & 63) * 128, m0v = ldsb + PF_OFF + w * 256;
  pf_load(m0v, vo, mk_rsrc(bid < 128 ? cr : ci, S_ * S_ * 2), ((bid & 127) * 8 + w) * 8192u);
  if (w == 7) pf_load(m0v, vo, mk_rsrc(wd, F_ * D_ * 2), bid * 8192u);
}

template <bool TR, bool MAIN>
__device__ __forceinline__ void p1_compute(char* smem, int wid, u16* __restrict__ outp, int rb, int ncol0, float sgn,
                                           const u16* cr, const u16* ci, const u16* wd, float* stat, int ct) {
  const int lane = threadIdx.x & 63, wm = wid >> 2, wn = wid & 3;
  int fr = lane & 15, fq = lane >> 4;
  const int rsw = (fq ^ ((fr >> 1) & 7)) << 4;
  int a_rd[4], b_rd[4];
#pragma unroll
  for (int i = 0; i < 4; ++i) a_rd[i] = (wm * 64 + (MAIN ? i : (i + wn) & 3) * 16 + fr) * 128 + rsw;
#pragma unroll
  for (int j = 0; j < 4; ++j) {
    const int ja = MAIN ? j : (j + 2 * wm) & 3;
    if constexpr (!TR) {
      b_rd[j] = K1_B_OFF + (wn * 64 + ja * 16 + fr) * 128 + rsw;
    } else {
      const int q = fr >> 2, pp = fr & 3, f = q | ((fq & 1) << 2);
      const int ch = 4 * wn + 2 * (ja & 1) + (pp >> 1);
      b_rd[j] = K1_B_OFF + (ja >> 1) * 16384 + (8 * fq + q) * 256 + ((ch ^ (f << 1)) << 4) + (pp & 1) * 8;
    }
  }
  f32x4 acc[4][4], acs[2], ars, agr;
#pragma unroll
  for (int j = 0; j < 4; ++j)
#pragma unroll
    for (int i = 0; i < 4; ++i) acc[j][i] = (f32x4){0.f, 0.f, 0.f, 0.f};
  acs[0] = acs[1] = ars = agr = (f32x4){0.f, 0.f, 0.f, 0.f};
  const bf16x8 ones = {0x3F80, 0x3F80, 0x3F80, 0x3F80, 0x3F80, 0x3F80, 0x3F80, 0x3F80};

  bf16x8 af0[4], bf0[4], af1[4], bf1[4];
  int sa = 0, sb = 0;
#define K1_LDA(dst, i, ks) dst[i] = *(const bf16x8*)(smem + sa + (a_rd[i] ^ ((ks) * 64)))
#define K1_LDB(dst, j, ks)                                                            \
  do {                                                                                \
    if constexpr (!TR) {                                                              \
      dst[j] = *(const bf16x8*)(smem + sb + (b_rd[j] ^ ((ks) * 64)));                 \
    } else {                                                                          \
      const char* p_ = smem + sb + b_rd[j] + (ks) * 32 * 256;                         \
      dst[j] = cat8(tr_read(p_), tr_read(p_ + 4 * 256));                              \
    }                                                                                 \
  } while (0)
#define K1_NEXT() do { sa = sa == 2 * 16384 ? 0 : sa + 16384; sb = sb == 2 * 32768 ? 0 : sb + 32768; } while (0)
#define K1_MMA4(j, af, bfr) _Pragma("unroll") for (int i_ = 0; i_ < 4; ++i_) acc[j][i_] = MFMA(bfr[j], af[i_], acc[j][i_])
#define K1_POS(afc, bfc, afn, bfn, RD, ks)                                            \
  do {                                                                                \
    if constexpr (MAIN) {                                                             \
      K1_MMA4(0, afc, bfc);                                                           \
      if (RD) { K1_LDA(afn, 0, ks); K1_LDA(afn, 1, ks); K1_LDB(bfn, 0, ks); }         \
      SB();                                                                           \
      K1_MMA4(1, afc, bfc);                                                           \
      if (RD) { K1_LDA(afn, 2, ks); K1_LDA(afn, 3, ks); K1_LDB(bfn, 1, ks); }         \
      SB();                                                                           \
      K1_MMA4(2, afc, bfc);                                                           \
      if (RD) { K1_LDB(bfn, 2, ks); K1_LDB(bfn, 3, ks); }                             \
      SB();                                                                           \
      K1_MMA4(3, afc, bfc);                                                           \
      SB();                                                                           \
    } else {                                                                          \
      if (RD) { K1_LDA(afn, 0, ks); K1_LDB(bfn, 0, ks); K1_LDB(bfn, 1, ks); }         \
      acs[0] = MFMA(bfc[0], ones, acs[0]);                                            \
      acs[1] = MFMA(bfc[1], ones, acs[1]);                                            \
      ars = MFMA(ones, afc[0], ars);                                                  \
      agr = MFMA(afc[0], afc[0], agr);                                                \
    }                                                                                 \
    if (ks) K1_NEXT();                                                                \
  } while (0)

  if constexpr (!MAIN) {
#pragma unroll 1
    for (int C = 0; C < 8; ++C) {
      BAR();
      K1_LDA(af0, 0, 0); K1_LDB(bf0, 0, 0); K1_LDB(bf0, 1, 0);
      K1_LDA(af1, 0, 1); K1_LDB(bf1, 0, 1); K1_LDB(bf1, 1, 1);
      K1_POS(af0, bf0, af1, bf1, false, 0);
      K1_POS(af1, bf1, af0, bf0, false, 1);
    }
  } else {
    BAR();
    K1_LDA(af0, 0, 0); K1_LDB(bf0, 0, 0); K1_LDB(bf0, 1, 0);
    K1_LDA(af0, 1, 0); K1_LDA(af0, 2, 0); K1_LDA(af0, 3, 0); K1_LDB(bf0, 2, 0); K1_LDB(bf0, 3, 0);
#pragma unroll 1
    for (int t = 0; t < 14; t += 2) {
      if (t == 6 + (wid & 3) * 2) warm_up((unsigned)(size_t)(LAS char*)smem, wid, cr, ci, wd);
      K1_POS(af0, bf0, af1, bf1, true, 1); BAR();
      K1_POS(af1, bf1, af0, bf0, true, 0); BAR();
    }
    asm volatile("" : "+s"(sa), "+s"(sb));
    K1_POS(af0, bf0, af1, bf1, true, 1);
    K1_POS(af1, bf1, af0, bf0, false, 0);
  }
  BAR();
  {
    int tz = threadIdx.x;
    asm volatile("" : "+v"(tz));
    fr = tz & 15; fq = (tz >> 4) & 3;
  }
  if constexpr (!MAIN) {
    const rsrc_t rs = mk_rsrc(stat, 31 * 1024);
    const float d = (fr & 2) ? ((fr & 1) ? agr[3] : agr[2]) : ((fr & 1) ? agr[1] : agr[0]);
    const float mean = ars[0] * (1.0f / D_);
    const float var = fmaxf(d * (1.0f / D_) - mean * mean, 0.f);
    if (fq == (fr >> 2)) {
      const unsigned o = rb * 1024 + (wm * 64 + wn * 16 + fr) * 4;
      __builtin_amdgcn_raw_buffer_store_b32(__builtin_bit_cast(unsigned, mean), rs, o, 0, 16);
      __builtin_amdgcn_raw_buffer_store_b32(__builtin_bit_cast(unsigned, __builtin_amdgcn_rsqf(var + 1e-5f)), rs, o + 512, 0, 16);
    }
    if (fr == 0 && rb < 15) {
      const unsigned o = (16 + ct) * 1024 + (wn * 64 + 2 * wm * 16 + fq * 4) * 4;
      __builtin_amdgcn_raw_buffer_store_b128(__builtin_bit_cast(u32x4, acs[0]), rs, o, 0, 16);
      __builtin_amdgcn_raw_buffer_store_b128(__builtin_bit_cast(u32x4, acs[1]), rs, o + 64, 0, 16);
    }
    wait_v<0>();
    BAR();
    warm_up((unsigned)(size_t)(LAS char*)smem, wid, cr, ci, wd);
    return;
  }
  const float* xs = (const float*)(smem + XS_OFF);
  f32x4 cs[4];
#pragma unroll
  for (int j = 0; j < 4; ++j) cs[j] = *(const f32x4*)(xs + 256 + wn * 64 + j * 16 + fq * 4);

  constexpr int ROWB = TR ? 64 + 16 : 128 + 16;
  char* stg = smem + STG_OFF + wid * (64 * (128 + 16));
  constexpr int LPR = TR ? 4 : 8;
  constexpr int RPI = 64 / LPR;
  const int l_ = fq * 16 + fr, r0 = l_ / LPR, c16 = l_ % LPR;
  const rsrc_t ro = mk_rsrc(outp, TR ? S_ * HLD * 2 : S_ * D_ * 2);
  const unsigned go = (unsigned)(rb * 128 + wm * 64 + r0) * (TR ? HLD * 2 : D_ * 2) + ncol0 * 2 + wn * (TR ? 64 : 128) + c16 * 16;
#pragma unroll
  for (int i = 0; i < 4; ++i) {
    const int ml = i * 16 + fr, mrow = wm * 64 + ml;
    const float mean = xs[mrow], rstd = xs[128 + mrow];
    if constexpr (!TR) {
      const float rs = rstd * sgn;
#pragma unroll
      for (int j = 0; j < 4; ++j) {
        const f32x4 v = (acc[j][i] - mean * cs[j]) * rs;
        u32x2 o;
        o[0] = pack_bf16(v[0], v[1]);
        o[1] = pack_bf16(v[2], v[3]);
        *(u32x2*)(stg + ml * ROWB + (j * 16 + fq * 4) * 2) = o;
      }
    } else {
      const bool valid = ncol0 + wn * 32 < F_;
#pragma unroll
      for (int jj = 0; jj < 2; ++jj) {
        u32x2 o = {0u, 0u};
        if (valid) {
          const float mr = -mean * rstd;
          const f32x4 v = geglu4(acc[jj][i] * rstd + mr * cs[jj], acc[2 + jj][i] * rstd + mr * cs[2 + jj]);
          o[0] = pack_bf16(v[0], v[1]);
          o[1] = pack_bf16(v[2], v[3]);
        }
        *(u32x2*)(stg + ml * ROWB + (jj * 16 + fq * 4) * 2) = o;
      }
    }
#pragma unroll
    for (int it = i * 16 / RPI; it < (i + 1) * 16 / RPI; ++it) {
      const u32x4 v = *(const u32x4*)(stg + (r0 + it * RPI) * ROWB + c16 * 16);
      __builtin_amdgcn_raw_buffer_store_b128(v, ro, go + it * RPI * (TR ? HLD * 2 : D_ * 2), 0, 16);
    }
  }
  wait_v<0>();
  BAR();
}

__global__ __launch_bounds__(768) void fused_kernel(const u16* __restrict__ x, const u16* __restrict__ cr,
                                                    const u16* __restrict__ ci, const u16* __restrict__ wr,
                                                    const u16* __restrict__ wi, const u16* __restrict__ wg,
                                                    const u16* __restrict__ wu, const u16* __restrict__ wd,
                                                    u16* __restrict__ ab, u16* __restrict__ h,
                                                    unsigned* __restrict__ slab, u64* __restrict__ flags,
                                                    float* __restrict__ stat,
                                                    u16* __restrict__ out) {
  extern __shared__ __attribute__((aligned(16))) char smem[];
  const int tid = threadIdx.x, lane = tid & 63, hw = __builtin_amdgcn_readfirstlane(tid >> 6), wid = hw < 4 ? hw + 8 : hw - 4;
  const unsigned ldsb = (unsigned)(size_t)(LAS char*)smem;
  const int bid = blockIdx.x;
  const bool has1 = bid < 240;
  const int u1 = ((bid & 7) >> 1) * 30 + (bid >> 3);
  const int ct = has1 ? u1 >> 3 : (bid < 255 ? bid - 240 : 14), rb = has1 ? (bid & 1) * 8 + (u1 & 7) : bid - 240;
  const int xcd = bid & 7, loc = bid >> 3;
  const int p = xcd >> 1;
  const int trow = (xcd & 1) * 8 + (loc >> 2), tcol = loc & 3;
  const int tile = trow * 4 + tcol;
  const bool full = p < 2;
  const int cb = p < 2 ? p * 22 : 44 + (p - 2) * 21;
  const int nch = full ? 22 : 21;
  const int rot = (trow * 5) % 21 - (full ? 0 : 1);

  if (wid >= 8) {
    const int lw = wid - 8;
    if (ct < 4) p1_loader<false>(ldsb, lw, lane, x, ct < 2 ? wr : wi, nullptr, rb, (ct & 1) * 256, ct, has1, stat, flags);
    else p1_loader<true>(ldsb, lw, lane, x, wg, wu, rb, (ct - 4) * 128, ct, has1, stat, flags);
    BAR();
    const unsigned avoff = (lane >> 3) * 4096u + (((lane & 7) ^ ((((lw & 1) << 2) + (lane >> 4)) & 7)) * 16);
    const unsigned bvoff = (lane >> 4) * 1024u + (((lane & 15) ^ (((lane >> 4) | (((lw >> 1) & 1) << 2)) << 1)) * 16);
    const unsigned asoff0 = (unsigned)(trow * 128 + lw * 8) * 4096u;
    const unsigned bsoff0 = (unsigned)tcol * 256u + lw * 4096u;
    const unsigned m0w = ldsb + lw * 1024;
    const int pfirst = full ? 0 : 1;
#define P2_CHUNK(pos) const int cr_ = (pos) + rot; const int c_ = cb + (cr_ >= nch ? cr_ - nch : cr_)
#define P2_A(slot)                                                                                            \
  do {                                                                                                        \
    const rsrc_t ra_ = mk_rsrc(c_ < 32 ? cr : (c_ < 64 ? ci : (const u16*)h), S_ * S_ * 2);                   \
    const unsigned sa_ = asoff0 + (unsigned)(c_ & 31) * 128u;                                                 \
    _Pragma("unroll") for (int i = 0; i < 4; ++i) dma16c(m0w + (slot) + i * 4096, avoff, ra_, sa_ + i * (32u * 4096u)); \
  } while (0)
#define P2_B(slot)                                                                                            \
  do {                                                                                                        \
    const rsrc_t rb_ = mk_rsrc(c_ < 64 ? (const u16*)ab : wd, c_ < 64 ? 2 * S_ * D_ * 2 : F_ * D_ * 2);       \
    const unsigned sb_ = bsoff0 + (unsigned)(c_ & 63) * 65536u;                                               \
    _Pragma("unroll") for (int i = 0; i < 4; ++i) dma16c(m0w + K2_B_OFF + (slot) + i * 4096, bvoff, rb_, sb_ + i * (16u * 1024u)); \
  } while (0)
    if (full) {
      for (int pos = 0; pos < K2_NS - 1; ++pos) { P2_CHUNK(pos); P2_A(pos * 16384); }
    }
    BAR();
    flags_wait(flags, bid, 0, 11, lane);
    if (!full) {
      for (int pos = 1; pos < K2_NS; ++pos) { P2_CHUNK(pos); P2_A(pos * 16384); }
    }
    for (int pos = pfirst; pos < pfirst + K2_NS - 1; ++pos) { P2_CHUNK(pos); P2_B(pos * 16384); }
    wait_v<4 * (K2_NS - 3)>();
    BAR();
    unsigned slot = ((pfirst + K2_NS - 1) & 3) * 16384;
    for (int pos = pfirst + K2_NS - 1; pos < 22 + K2_NS - 3; ++pos) {
      if (pos < 22) {
        P2_CHUNK(pos);
        P2_A(slot);
        P2_B(slot);
        slot = (slot + 16384) & (K2_NS * 16384 - 1);
      }
      if (pos < 22) wait_v<8 * (K2_NS - 3)>();
      else wait_v<0>();
      BAR();
    }
    return;
  }

  if (has1) {
    if (ct < 4) p1_compute<false, true>(smem, wid, ab + (ct < 2 ? 0 : (size_t)S_ * D_), rb, (ct & 1) * 256, ct < 2 ? 1.0f : -1.0f, cr, ci, wd, stat, ct);
    else p1_compute<true, true>(smem, wid, h, rb, (ct - 4) * 128, 1.0f, cr, ci, wd, stat, ct);
  } else {
    if (ct < 4) p1_compute<false, false>(smem, wid, nullptr, rb, (ct & 1) * 256, 1.0f, cr, ci, wd, stat, ct);
    else p1_compute<true, false>(smem, wid, nullptr, rb, (ct - 4) * 128, 1.0f, cr, ci, wd, stat, ct);
  }
  if (wid == 0) {
    int lane = threadIdx.x;
    asm volatile("" : "+v"(lane));
    lane &= 63;
    if (!has1) {
      const int crb = lane < 16 ? rb : lane - 16, cct = lane < 16 ? lane : ct;
      const int u = cct * 8 + (crb & 7);
      const int dst = (u % 30) * 8 + 2 * (u / 30) + (crb >> 3);
      if (lane < 15) flag_set(flags, dst, 14);
      else if (lane >= 16 && lane < 32 && rb < 15) flag_set(flags, dst, 15);
    } else if (ct < 4) {
      const int g = (ct >> 1) * 16 + rb, pq = g < 11 ? 0 : (g < 22 ? 1 : 2);
      if (lane < 32) flag_set(flags, cons_bid(pq, lane >> 1, 2 * (ct & 1) + (lane & 1)), g - 11 * pq);
    } else {
      if (lane < 4) flag_set(flags, cons_bid(3, rb, lane), ct - 4);
      else if (lane < 8 && ct == 4) flag_set(flags, cons_bid(2, rb, lane - 4), 10);
    }
  }
  int lz = threadIdx.x;
  asm volatile("" : "+v"(lz));
  const int ln = lz & 63, fr = ln & 15, fq = ln >> 4;
  const int kg = wid >> 2, wt = (wid >> 1) & 1, we = wid & 1, w4 = wid & 3;
  const int asw = (fr >> 1) & 7;
  int a_rd[4];
#pragma unroll
  for (int i = 0; i < 4; ++i) a_rd[i] = (wt * 64 + ((i + p) & 3) * 16 + fr) * 128 + (((fq + 4 * kg) ^ asw) << 4);
  int b_rd[4];
  {
    const int q = fr >> 2, pp = fr & 3, f = q | ((fq & 1) << 2);
#pragma unroll
    for (int j = 0; j < 4; ++j) {
      const int ch = 8 * we + 2 * j + (pp >> 1);
      b_rd[j] = K2_B_OFF + (32 * kg + 8 * fq + q) * 256 + ((ch ^ (f << 1)) << 4) + (pp & 1) * 8;
    }
  }
  f32x4 acc[4][4];
#pragma unroll
  for (int j = 0; j < 4; ++j)
#pragma unroll
    for (int i = 0; i < 4; ++i) acc[j][i] = (f32x4){0.f, 0.f, 0.f, 0.f};
  bf16x8 af0[4], bf0[4], af1[4], bf1[4];
#define LDA(dst, i, so) dst[i] = *(const bf16x8*)(smem + (so) + a_rd[i])
#define LDB(dst, j, so)                                                               \
  do {                                                                                \
    const char* pb_ = smem + (so) + b_rd[j];                                          \
    dst[j] = cat8(tr_read(pb_), tr_read(pb_ + 4 * 256));                              \
  } while (0)
#define MMA4(j, af, bfr) _Pragma("unroll") for (int i_ = 0; i_ < 4; ++i_) acc[j][i_] = MFMA(bfr[j], af[i_], acc[j][i_])
#define K2_POS(afc, bfc, afn, bfn, RD)                                                \
  do {                                                                                \
    MMA4(0, afc, bfc);                                                                \
    if (RD) { LDA(afn, 0, s1); LDA(afn, 1, s1); LDB(bfn, 0, s1); }                    \
    SB();                                                                             \
    MMA4(1, afc, bfc);                                                                \
    if (RD) { LDA(afn, 2, s1); LDA(afn, 3, s1); LDB(bfn, 1, s1); }                    \
    SB();                                                                             \
    MMA4(2, afc, bfc);                                                                \
    if (RD) { LDB(bfn, 2, s1); LDB(bfn, 3, s1); }                                     \
    SB();                                                                             \
    MMA4(3, afc, bfc);                                                                \
    SB();                                                                             \
    s1 = (s1 + 16384) & (K2_NS * 16384 - 1);                                          \
  } while (0)
  BAR();
  int s1;
  if (full) {
    LDA(af0, 0, 0); LDA(af0, 1, 0); LDA(af0, 2, 0); LDA(af0, 3, 0);
    LDB(bf0, 0, 0); LDB(bf0, 1, 0); LDB(bf0, 2, 0); LDB(bf0, 3, 0);
    s1 = 16384;
    K2_POS(af0, bf0, af1, bf1, true);
    BAR();
  } else {
    LDA(af1, 0, 16384); LDA(af1, 1, 16384); LDA(af1, 2, 16384); LDA(af1, 3, 16384);
    LDB(bf1, 0, 16384); LDB(bf1, 1, 16384); LDB(bf1, 2, 16384); LDB(bf1, 3, 16384);
    s1 = 2 * 16384;
  }
#pragma unroll 1
  for (int j = 1; j < 19; j += 2) {
    K2_POS(af1, bf1, af0, bf0, true); BAR();
    K2_POS(af0, bf0, af1, bf1, true); BAR();
  }
  K2_POS(af1, bf1, af0, bf0, true); BAR();
  K2_POS(af0, bf0, af1, bf1, true);
  K2_POS(af1, bf1, af0, bf0, false);

  {
    char* xb = smem + (2 + (w4 & 1)) * 16384 + (w4 >> 1) * K2_B_OFF + ln * 16;
#pragma unroll
    for (int jj = 0; jj < 2; ++jj)
#pragma unroll
      for (int i = 0; i < 4; ++i) {
        const f32x4 v = kg ? acc[jj][i] : acc[2 + jj][i];
        *(f32x4*)(xb + kg * 8192 + (jj * 4 + i) * 1024) = v;
      }
    wait_l0();
    BAR();
#pragma unroll
    for (int jj = 0; jj < 2; ++jj)
#pragma unroll
      for (int i = 0; i < 4; ++i) {
        const f32x4 o = *(const f32x4*)(xb + (kg ^ 1) * 8192 + (jj * 4 + i) * 1024);
        const f32x4 mine = kg ? acc[2 + jj][i] : acc[jj][i];
        acc[jj][i] = mine + o;
      }
  }
  const rsrc_t rs = mk_rsrc(slab, 64 * 16 * 8192);
  const unsigned lane_off = (unsigned)(wid * 64 + ln) * 16;
#pragma unroll
  for (int i = 1; i < 4; ++i) {
    const unsigned dstq = (i + p) & 3;
    const unsigned off = ((tile * 4 + dstq) * 4 + p) * 8192u + lane_off;
    u32x4 v;
    v[0] = pack_bf16(acc[0][i][0], acc[0][i][1]);
    v[1] = pack_bf16(acc[0][i][2], acc[0][i][3]);
    v[2] = pack_bf16(acc[1][i][0], acc[1][i][1]);
    v[3] = pack_bf16(acc[1][i][2], acc[1][i][3]);
    __builtin_amdgcn_raw_buffer_store_b128(v, rs, off, 0, 16);
  }
  const int trw = trow * 128 + wt * 64 + p * 16 + fr;
  const int ecol = tcol * 128 + we * 64 + kg * 32 + fq * 4;
  u32x2 xr[2];
#pragma unroll
  for (int jj = 0; jj < 2; ++jj) xr[jj] = *(const u32x2*)(x + (size_t)trw * D_ + ecol + jj * 16);
  wait_v<0>();
  BAR();
  if (wid == 0) {
    if (ln >= 1 && ln < 4) flag_set(flags, cons_bid((ln + p) & 3, trow, tcol), 14 - ln);
    flags_wait(flags, bid, 11, 3, ln);
    if (ln < 16) __hip_atomic_store((GAS u64*)(flags + bid * 16 + ln), 0ull, __ATOMIC_RELAXED, __HIP_MEMORY_SCOPE_AGENT);
  }
  BAR();
  __builtin_amdgcn_fence(__ATOMIC_ACQUIRE, "wavefront");

  u32x4 pv[3];
#pragma unroll
  for (int s = 1; s < 4; ++s) {
    const unsigned srcq = (s + p) & 3;
    pv[s - 1] = __builtin_amdgcn_raw_buffer_load_b128(rs, ((tile * 4 + p) * 4 + srcq) * 8192u + lane_off, 0, 16);
  }
#pragma unroll
  for (int jj = 0; jj < 2; ++jj) {
    f32x4 v = acc[jj][0];
#pragma unroll
    for (int s = 0; s < 3; ++s) {
      v[0] += bf_lo(pv[s][2 * jj]);
      v[1] += bf_hi(pv[s][2 * jj]);
      v[2] += bf_lo(pv[s][2 * jj + 1]);
      v[3] += bf_hi(pv[s][2 * jj + 1]);
    }
    u32x2 o;
    o[0] = pack_bf16(v[0] + bf_lo(xr[jj][0]), v[1] + bf_hi(xr[jj][0]));
    o[1] = pack_bf16(v[2] + bf_lo(xr[jj][1]), v[3] + bf_hi(xr[jj][1]));
    *(u32x2*)(out + (size_t)trw * D_ + ecol + jj * 16) = o;
  }
}

extern "C" void kernel_launch(void* const* d_in, const int* in_sizes, int n_in, void* d_out, int out_size,
                              void* d_ws, size_t ws_size, hipStream_t stream) {
  char* ws = (char*)d_ws;
  (void)hipFuncSetAttribute((const void*)fused_kernel, hipFuncAttributeMaxDynamicSharedMemorySize, LDS_BYTES);
  fused_kernel<<<256, 768, LDS_BYTES, stream>>>(
      (const u16*)d_in[0], (const u16*)d_in[1], (const u16*)d_in[2], (const u16*)d_in[3], (const u16*)d_in[4],
      (const u16*)d_in[6], (const u16*)d_in[7], (const u16*)d_in[8], (u16*)(ws + WS_AB), (u16*)(ws + WS_H),
      (unsigned*)(ws + WS_SLAB), (u64*)(ws + WS_FLAG), (float*)(ws + WS_STAT), (u16*)d_out);
}
```
